```python
import torch

def _softmax(x, axis=-1):
    return torch.softmax(x, dim=axis)
def _gelu(x):
    return torch.nn.functional.gelu(x, approximate="tanh")
def _silu(x):
    return torch.nn.functional.silu(x)
def _sigmoid(x):
    return torch.sigmoid(x)
def _layernorm(x, axis=-1, eps=1e-5):
    m = x.mean(dim=axis, keepdim=True)
    v = x.var(dim=axis, keepdim=True, unbiased=False)
    return (x - m) / torch.sqrt(v + eps)
def _scatter_add(tgt, idx, src, axis=0):
    return tgt.clone().index_add_(axis, idx, src)
def _segment_sum(src, seg, n):
    out = torch.zeros((n,) + src.shape[1:], dtype=src.dtype)
    return out.index_add_(0, seg, src)
def _pad(x, pw):
    flat = tuple(v for pair in reversed(pw) for v in pair)
    return torch.nn.functional.pad(x, flat)

kernel_name = 'standalone__gqa_softmax__near_ridge'

def setup_inputs():

    _w = (lambda a: (lambda t: t.to(torch.bfloat16) if t.is_floating_point() else t)(torch.as_tensor(a)))
    B, Hq, Hkv, Sq, Sk, D = 16, 32, 32, 368, 512, 192
    return {
        "q": _w((lambda *s: torch.randn(*s, dtype=torch.bfloat16) * 0.8)(B, Hq, Sq, D)),
        "k": _w((lambda *s: torch.randn(*s, dtype=torch.bfloat16) * 0.8)(B, Hkv, Sk, D)),
        "v": _w((lambda *s: torch.randn(*s, dtype=torch.bfloat16) * 0.05)(B, Hkv, Sk, D)),
    }

def reference(q, k, v):
    B, Hq, Sq, D = q.shape
    Hkv = k.shape[1]
    rep = Hq // Hkv
    k = (lambda x, n, axis: torch.repeat_interleave(x, n, dim=axis))(k, rep, axis=1)
    v = (lambda x, n, axis: torch.repeat_interleave(x, n, dim=axis))(v, rep, axis=1)
    scale = D ** -0.5
    scores = torch.einsum("bhqd,bhkd->bhqk", q, k) * scale
    p = _softmax(scores, axis=-1)
    return torch.einsum("bhqk,bhkd->bhqd", p, v)
```

```cpp
#include <hip/hip_runtime.h>
#include <hip/hip_bf16.h>
#include <stdint.h>

typedef float    f32x4  __attribute__((ext_vector_type(4)));
typedef float    f32x2  __attribute__((ext_vector_type(2)));
typedef __bf16   bf16x8 __attribute__((ext_vector_type(8)));
typedef __bf16   bf16x4 __attribute__((ext_vector_type(4)));
typedef __bf16   bf16x2 __attribute__((ext_vector_type(2)));
typedef short    s16x4  __attribute__((ext_vector_type(4)));
typedef unsigned u32x4  __attribute__((ext_vector_type(4)));
typedef unsigned u32x2  __attribute__((ext_vector_type(2)));

constexpr int NB = 16, NH = 32, SQ = 368, SK = 512, D = 192;
constexpr int ROWB = D * 2;
constexpr int SLOT = 32 * ROWB;
constexpr int NQT = 2;

#define DI __device__ __forceinline__
DI unsigned rfl(unsigned x) { return __builtin_amdgcn_readfirstlane(x); }
DI unsigned cvtpk(float lo, float hi) { f32x2 v = {lo, hi}; bf16x2 b = __builtin_convertvector(v, bf16x2); return __builtin_bit_cast(unsigned, b); }

template <int OFF> DI u32x2 tr_read(unsigned addr) {
  u32x2 r; asm volatile("ds_read_b64_tr_b16 %0, %1 offset:%2" : "=v"(r) : "v"(addr), "i"(OFF) : "memory"); return r;
}
template <int OFF> DI u32x4 rd128(unsigned addr) {
  u32x4 r; asm volatile("ds_read_b128 %0, %1 offset:%2" : "=v"(r) : "v"(addr), "i"(OFF) : "memory"); return r;
}

__global__ __launch_bounds__(256, 2)
void attn_proto1(const unsigned short* __restrict__ Q, const unsigned short* __restrict__ K, const unsigned short* __restrict__ V,
                 unsigned short* __restrict__ O) {
  extern __shared__ __attribute__((aligned(16))) char lds[];
  const unsigned tid = threadIdx.x, lane = tid & 63u;
  const unsigned wid = rfl(tid >> 6);
  const unsigned bid = blockIdx.x;
  const unsigned xcd = bid & 7u, idx = bid >> 3;
  const unsigned head = xcd * 64u + idx / 3u, part = idx % 3u;
  const unsigned row0 = part * 128u + wid * 32u;
  const unsigned g = lane >> 4, r = lane & 15u, r7 = r & 7u;
  const unsigned ldsb = (unsigned)(size_t)lds;

  __amdgpu_buffer_rsrc_t srdK = __builtin_amdgcn_make_buffer_rsrc((void*)(K + (size_t)head * SK * D), (short)0, SK * ROWB, 0x00020000);
  __amdgpu_buffer_rsrc_t srdV = __builtin_amdgcn_make_buffer_rsrc((void*)(V + (size_t)head * SK * D), (short)0, SK * ROWB, 0x00020000);
  __amdgpu_buffer_rsrc_t srdQ = __builtin_amdgcn_make_buffer_rsrc((void*)(Q + (size_t)head * SQ * D), (short)0, SQ * ROWB, 0x00020000);
  __amdgpu_buffer_rsrc_t srdO = __builtin_amdgcn_make_buffer_rsrc((void*)(O + (size_t)head * SQ * D), (short)0, SQ * ROWB, 0x00020000);

  const unsigned drr = (lane >> 2) & 7u;
  const unsigned dsrc = drr * ROWB + 64u * (lane >> 5) + 16u * ((lane & 3u) ^ (2u * (drr >> 2)));
  const unsigned KL = 1024u * (r >> 3) + 64u * r7 + 16u * (g ^ (2u * (r7 >> 2)));
  const unsigned q4 = (lane >> 2) & 3u, p = lane & 3u;
  const unsigned V0 = 1024u * (g >> 1) + 64u * (4u * (g & 1u) + q4) + 32u * (g & 1u) + 8u * p;

  bf16x8 qf[NQT][6];
#pragma unroll
  for (int qt = 0; qt < NQT; ++qt)
#pragma unroll
    for (int s = 0; s < 6; ++s) {
      u32x4 w = __builtin_amdgcn_raw_buffer_load_b128(srdQ, (row0 + 16u * qt + r) * ROWB + 64u * s + 16u * g, 0, 0);
      qf[qt][s] = __builtin_bit_cast(bf16x8, w);
    }
  auto dma = [&](int t) {
    const unsigned kb = ldsb + (unsigned)(t & 1) * 2u * SLOT, vb = kb + SLOT;
#pragma unroll
    for (int L = 0; L < 3; ++L) {
      const unsigned so = (unsigned)t * SLOT + wid * 8u * ROWB + 128u * L;
      const unsigned dk = kb + 1024u * (4u * L + wid), dv = vb + 1024u * (4u * L + wid);
      asm volatile("s_mov_b32 m0, %0\n\ts_nop 0\n\tbuffer_load_dwordx4 %1, %2, %3 offen lds" :: "s"(dk), "v"(dsrc), "s"(srdK), "s"(so) : "memory");
      asm volatile("s_mov_b32 m0, %0\n\ts_nop 0\n\tbuffer_load_dwordx4 %1, %2, %3 offen lds" :: "s"(dv), "v"(dsrc), "s"(srdV), "s"(so) : "memory");
    }
  };
  f32x4 o[NQT][12];
#pragma unroll
  for (int qt = 0; qt < NQT; ++qt)
#pragma unroll
    for (int dt = 0; dt < 12; ++dt) o[qt][dt] = f32x4{0.f, 0.f, 0.f, 0.f};
  float m[NQT], l[NQT];
#pragma unroll
  for (int qt = 0; qt < NQT; ++qt) { m[qt] = -1e30f; l[qt] = 0.f; }
  const float C = 0.07216878364870322f * 1.4426950408889634f;

  dma(0);
  asm volatile("s_waitcnt vmcnt(0)\n\ts_barrier" ::: "memory");
  for (int t = 0; t < 16; ++t) {
    if (t + 1 < 16) dma(t + 1);
    const unsigned kb = ldsb + (unsigned)(t & 1) * 2u * SLOT, vb = kb + SLOT;
    const unsigned ka = kb + KL, va0 = vb + V0, va1 = va0 ^ 32u;
    f32x4 sacc[NQT][2];
#pragma unroll
    for (int qt = 0; qt < NQT; ++qt) { sacc[qt][0] = f32x4{0.f, 0.f, 0.f, 0.f}; sacc[qt][1] = f32x4{0.f, 0.f, 0.f, 0.f}; }
    u32x4 kf[12];
#define KR(s, kt) kf[2 * (s) + (kt)] = rd128<4096 * ((s) >> 1) + 2048 * (kt) + 512 * ((s) & 1)>(ka)
    KR(0, 0); KR(0, 1); KR(1, 0); KR(1, 1); KR(2, 0); KR(2, 1); KR(3, 0); KR(3, 1); KR(4, 0); KR(4, 1); KR(5, 0); KR(5, 1);
#undef KR
    asm volatile("s_waitcnt lgkmcnt(0)" : "+v"(kf[0]), "+v"(kf[1]), "+v"(kf[2]), "+v"(kf[3]), "+v"(kf[4]), "+v"(kf[5]),
                                           "+v"(kf[6]), "+v"(kf[7]), "+v"(kf[8]), "+v"(kf[9]), "+v"(kf[10]), "+v"(kf[11]) :: "memory");
#pragma unroll
    for (int s = 0; s < 6; ++s)
#pragma unroll
      for (int kt = 0; kt < 2; ++kt)
#pragma unroll
        for (int qt = 0; qt < NQT; ++qt)
          sacc[qt][kt] = __builtin_amdgcn_mfma_f32_16x16x32_bf16(__builtin_bit_cast(bf16x8, kf[2 * s + kt]), qf[qt][s], sacc[qt][kt], 0, 0, 0);
    u32x2 vf[12][2];
#define VR(dt, hh) vf[dt][hh] = tr_read<4096 * ((dt) >> 2) + 2048 * (hh) + 512 * (((dt) >> 1) & 1)>(((dt) & 1) ? va1 : va0)
#define VR2(dt) VR(dt, 0); VR(dt, 1)
    VR2(0); VR2(1); VR2(2); VR2(3); VR2(4); VR2(5); VR2(6); VR2(7); VR2(8); VR2(9); VR2(10); VR2(11);
#undef VR2
#undef VR
    bf16x8 pf[NQT];
#pragma unroll
    for (int qt = 0; qt < NQT; ++qt) {
      float mx = fmaxf(fmaxf(fmaxf(sacc[qt][0][0], sacc[qt][0][1]), fmaxf(sacc[qt][0][2], sacc[qt][0][3])),
                       fmaxf(fmaxf(sacc[qt][1][0], sacc[qt][1][1]), fmaxf(sacc[qt][1][2], sacc[qt][1][3])));
      mx = fmaxf(mx, __shfl_xor(mx, 16));
      mx = fmaxf(mx, __shfl_xor(mx, 32));
      const float mn = fmaxf(m[qt], mx);
      const float alpha = __builtin_amdgcn_exp2f((m[qt] - mn) * C);
      m[qt] = mn;
      const float nm = -mn * C;
      float pv[8]; float ps = 0.f;
#pragma unroll
      for (int j = 0; j < 8; ++j) { pv[j] = __builtin_amdgcn_exp2f(fmaf(sacc[qt][j >> 2][j & 3], C, nm)); ps += pv[j]; }
      l[qt] = l[qt] * alpha + ps;
      u32x4 w = {cvtpk(pv[0], pv[1]), cvtpk(pv[2], pv[3]), cvtpk(pv[4], pv[5]), cvtpk(pv[6], pv[7])};
      pf[qt] = __builtin_bit_cast(bf16x8, w);
#pragma unroll
      for (int dt = 0; dt < 12; ++dt) o[qt][dt] *= alpha;
    }
    asm volatile("s_waitcnt lgkmcnt(0)" : "+v"(vf[0][0]), "+v"(vf[0][1]), "+v"(vf[1][0]), "+v"(vf[1][1]), "+v"(vf[2][0]), "+v"(vf[2][1]),
                                           "+v"(vf[3][0]), "+v"(vf[3][1]), "+v"(vf[4][0]), "+v"(vf[4][1]), "+v"(vf[5][0]), "+v"(vf[5][1]) :: "memory");
    asm volatile("" : "+v"(vf[6][0]), "+v"(vf[6][1]), "+v"(vf[7][0]), "+v"(vf[7][1]), "+v"(vf[8][0]), "+v"(vf[8][1]),
                      "+v"(vf[9][0]), "+v"(vf[9][1]), "+v"(vf[10][0]), "+v"(vf[10][1]), "+v"(vf[11][0]), "+v"(vf[11][1]) :: "memory");
#pragma unroll
    for (int dt = 0; dt < 12; ++dt) {
      u32x4 w = {vf[dt][0][0], vf[dt][0][1], vf[dt][1][0], vf[dt][1][1]};
      bf16x8 a = __builtin_bit_cast(bf16x8, w);
#pragma unroll
      for (int qt = 0; qt < NQT; ++qt) o[qt][dt] = __builtin_amdgcn_mfma_f32_16x16x32_bf16(a, pf[qt], o[qt][dt], 0, 0, 0);
    }
    asm volatile("s_waitcnt vmcnt(0)\n\ts_barrier" ::: "memory");
  }
#pragma unroll
  for (int qt = 0; qt < NQT; ++qt) {
    float ls = l[qt];
    ls += __shfl_xor(ls, 16);
    ls += __shfl_xor(ls, 32);
    const float inv = 1.f / ls;
#pragma unroll
    for (int dt = 0; dt < 12; ++dt) {
      u32x2 w = {cvtpk(o[qt][dt][0] * inv, o[qt][dt][1] * inv), cvtpk(o[qt][dt][2] * inv, o[qt][dt][3] * inv)};
      __builtin_amdgcn_raw_buffer_store_b64(w, srdO, (row0 + 16u * qt + r) * ROWB + 32u * dt + 8u * g, 0, 0);
    }
  }
}

extern "C" void kernel_launch(void* const* d_in, const int* in_sizes, int n_in, void* d_out, int out_size, void* d_ws, size_t ws_size,
                              hipStream_t stream) {
  (void)in_sizes; (void)n_in; (void)out_size; (void)d_ws; (void)ws_size;
  hipLaunchKernelGGL(attn_proto1, dim3(NB * NH * 3), dim3(256), 4 * SLOT, stream,
                     (const unsigned short*)d_in[0], (const unsigned short*)d_in[1], (const unsigned short*)d_in[2], (unsigned short*)d_out);
}
```

```cpp
#include <hip/hip_runtime.h>

extern "C" __global__ __launch_bounds__(512, 2)
void attn_h(const char* __restrict__ Q, const char* __restrict__ K, const char* __restrict__ V, char* __restrict__ O) {
  __shared__ __attribute__((aligned(256))) char lds[163840];
  const unsigned wid = __builtin_amdgcn_readfirstlane(threadIdx.x >> 6);
  const size_t h0 = (size_t)blockIdx.x * 2;
  const char* q = Q + h0 * (368 * 384);
  const char* k = K + h0 * (512 * 384);
  const char* v = V + h0 * (512 * 384);
  char* o = O + h0 * (368 * 384);
  const unsigned lb = (unsigned)(size_t)lds;
  asm volatile(
  "s_mov_b32 s59, %4\n"
  "s_mov_b32 s66, %5\n"
  "s_mov_b64 s[40:41], %0\n"
  "s_mov_b64 s[44:45], %3\n"
  "s_cmp_lt_u32 s59, 4\n"
  "s_cselect_b64 s[36:37], %1, %2\n"
  "s_and_b32 s37, s37, 0xffff\n"
  "s_mov_b32 s39, 0x20000\n"
  "s_and_b32 s41, s41, 0xffff\n"
  "s_mov_b32 s43, 0x20000\n"
  "s_and_b32 s45, s45, 0xffff\n"
  "s_mov_b32 s47, 0x20000\n"
  "s_mov_b32 s38, 393216\n"
  "s_mov_b32 s42, 282624\n"
  "s_mov_b32 s46, 141312\n"
  "s_mov_b32 s60, 12288\n"
  "s_mov_b32 s61, 4294930432\n"
  "s_mov_b32 s52, 0x3dd53b95\n"
  "s_mov_b32 s53, 0x43800000\n"
  "s_mov_b32 s65, 136\n"
  "s_mul_i32 s56, s59, 18432\n"
  "s_add_u32 s75, s56, 6144\n"
  "s_add_u32 s76, s56, 12288\n"
  "s_mov_b32 s72, s56\n"
  "s_mov_b32 s77, s75\n"
  "s_mov_b32 s78, s76\n"
  "s_mov_b32 s82, 0x3f803f80\n"
  "s_mov_b32 s80, s52\n"
  "s_mov_b32 s81, s52\n"
  "s_lshl_b32 s73, s59, 13\n"
  "s_add_u32 s73, s73, 141312\n"
  "s_and_b32 s62, s59, 3\n"
  "s_mov_b32 s79, 0\n"
  "s_add_u32 s63, s59, 0\n"
  "s_min_u32 s63, s63, 17\n"
  "s_mul_i32 s63, s63, 47\n"
  "s_lshr_b32 s63, s63, 6\n"
  "s_add_u32 s63, s63, 2\n"
  "s_bitset1_b32 s79, s63\n"
  "s_add_u32 s63, s59, 8\n"
  "s_min_u32 s63, s63, 17\n"
  "s_mul_i32 s63, s63, 47\n"
  "s_lshr_b32 s63, s63, 6\n"
  "s_add_u32 s63, s63, 2\n"
  "s_bitset1_b32 s79, s63\n"
  "s_add_u32 s63, s59, 16\n"
  "s_min_u32 s63, s63, 17\n"
  "s_mul_i32 s63, s63, 47\n"
  "s_lshr_b32 s63, s63, 6\n"
  "s_add_u32 s63, s63, 2\n"
  "s_bitset1_b32 s79, s63\n"
  "s_or_b32 s79, s79, 0x80008000\n"
  "s_mov_b32 s83, 0x60000\n"
  "s_mul_i32 s57, s59, 6144\n"
  "s_add_u32 s57, s57, s66\n"
  "s_add_u32 s57, s57, 98304\n"
  "s_mul_i32 s58, s59, 2304\n"
  "s_add_u32 s58, s58, s66\n"
  "s_add_u32 s58, s58, 141312\n"
  "s_mul_i32 s48, s62, 3072\n"
  "s_lshl_b32 s49, s62, 10\n"
  "s_add_u32 s49, s49, s66\n"
  "s_cmp_lt_u32 s59, 4\n"
  "s_cselect_b32 s63, 0, 49152\n"
  "s_add_u32 s49, s49, s63\n"
  "s_mov_b32 s50, 0\n"
  "v_mbcnt_lo_u32_b32 v12, -1, 0\n"
  "v_mbcnt_hi_u32_b32 v12, -1, v12\n"
  "v_bfe_u32 v13, v12, 2, 3\n"
  "v_mul_u32_u24_e32 v4, 384, v13\n"
  "v_lshrrev_b32_e32 v14, 5, v12\n"
  "v_lshl_add_u32 v4, v14, 6, v4\n"
  "v_bfe_u32 v14, v12, 4, 1\n"
  "v_lshlrev_b32_e32 v14, 1, v14\n"
  "v_and_b32_e32 v15, 3, v12\n"
  "v_xor_b32_e32 v14, v14, v15\n"
  "v_lshl_add_u32 v4, v14, 4, v4\n"
  "v_bfe_u32 v13, v12, 3, 1\n"
  "v_lshlrev_b32_e32 v0, 10, v13\n"
  "v_and_b32_e32 v13, 7, v12\n"
  "v_lshl_add_u32 v0, v13, 6, v0\n"
  "v_bfe_u32 v13, v12, 2, 1\n"
  "v_lshlrev_b32_e32 v13, 1, v13\n"
  "v_lshrrev_b32_e32 v14, 4, v12\n"
  "v_xor_b32_e32 v13, v13, v14\n"
  "v_lshl_add_u32 v0, v13, 4, v0\n"
  "v_add_u32_e32 v3, s57, v0\n"
  "v_add_u32_e32 v0, s66, v0\n"
  "v_lshrrev_b32_e32 v13, 5, v12\n"
  "v_lshlrev_b32_e32 v1, 10, v13\n"
  "v_bfe_u32 v13, v12, 2, 3\n"
  "v_lshl_add_u32 v1, v13, 6, v1\n"
  "v_bfe_u32 v13, v12, 4, 1\n"
  "v_lshl_add_u32 v1, v13, 5, v1\n"
  "v_and_b32_e32 v13, 3, v12\n"
  "v_lshl_add_u32 v1, v13, 3, v1\n"
  "s_add_u32 s62, s66, 86016\n"
  "v_add_u32_e32 v1, s62, v1\n"
  "v_xor_b32_e32 v2, 32, v1\n"
  "v_lshlrev_b32_e32 v5, 7, v12\n"
  "s_cmp_eq_u32 s59, 7\n"
  "s_cbranch_scc1 .LH_w7\n"
  "s_cmp_lt_u32 s59, 4\n"
  "s_cbranch_scc0 .LH_pBa\n"
  "s_add_u32 m0, s49, 0\n"
  "s_nop 0\n"
  "buffer_load_dwordx4 v4, s[36:39], s48 offen offset:0 nt lds\n"
  "s_add_u32 m0, s49, 3968\n"
  "s_nop 0\n"
  "buffer_load_dwordx4 v4, s[36:39], s48 offen offset:128 nt lds\n"
  "s_add_u32 m0, s49, 7936\n"
  "s_nop 0\n"
  "buffer_load_dwordx4 v4, s[36:39], s48 offen offset:256 nt lds\n"
  "s_add_u32 s49, s49, s60\n"
  "s_add_u32 s48, s48, s60\n"
  "buffer_load_dwordx4 v[64:67], v4, s[40:43], s72 offen offset:0 nt\n"
  "buffer_load_dwordx4 v[68:71], v4, s[40:43], s72 offen offset:3072 nt\n"
  "buffer_load_dwordx4 v[88:91], v4, s[40:43], s77 offen offset:0 nt\n"
  "buffer_load_dwordx4 v[92:95], v4, s[40:43], s77 offen offset:3072 nt\n"
  "s_add_u32 m0, s57, 0\n"
  "s_nop 0\n"
  "buffer_load_dwordx4 v4, s[40:43], s78 offen offset:0 nt lds\n"
  "s_add_u32 m0, s57, 4294965248\n"
  "s_nop 0\n"
  "buffer_load_dwordx4 v4, s[40:43], s78 offen offset:3072 nt lds\n"
  "buffer_load_dwordx4 v[72:75], v4, s[40:43], s72 offen offset:128 nt\n"
  "buffer_load_dwordx4 v[76:79], v4, s[40:43], s72 offen offset:3200 nt\n"
  "buffer_load_dwordx4 v[96:99], v4, s[40:43], s77 offen offset:128 nt\n"
  "buffer_load_dwordx4 v[100:103], v4, s[40:43], s77 offen offset:3200 nt\n"
  "s_add_u32 m0, s57, 1920\n"
  "s_nop 0\n"
  "buffer_load_dwordx4 v4, s[40:43], s78 offen offset:128 nt lds\n"
  "s_add_u32 m0, s57, 4294967168\n"
  "s_nop 0\n"
  "buffer_load_dwordx4 v4, s[40:43], s78 offen offset:3200 nt lds\n"
  "buffer_load_dwordx4 v[80:83], v4, s[40:43], s72 offen offset:256 nt\n"
  "buffer_load_dwordx4 v[84:87], v4, s[40:43], s72 offen offset:3328 nt\n"
  "buffer_load_dwordx4 v[104:107], v4, s[40:43], s77 offen offset:256 nt\n"
  "buffer_load_dwordx4 v[108:111], v4, s[40:43], s77 offen offset:3328 nt\n"
  "s_add_u32 m0, s57, 3840\n"
  "s_nop 0\n"
  "buffer_load_dwordx4 v4, s[40:43], s78 offen offset:256 nt lds\n"
  "s_add_u32 m0, s57, 1792\n"
  "s_nop 0\n"
  "buffer_load_dwordx4 v4, s[40:43], s78 offen offset:3328 nt lds\n"
  "s_branch .LH_pCa\n"
  ".LH_pBa:\n"
  "buffer_load_dwordx4 v[64:67], v4, s[40:43], s72 offen offset:0 nt\n"
  "buffer_load_dwordx4 v[68:71], v4, s[40:43], s72 offen offset:3072 nt\n"
  "buffer_load_dwordx4 v[88:91], v4, s[40:43], s77 offen offset:0 nt\n"
  "buffer_load_dwordx4 v[92:95], v4, s[40:43], s77 offen offset:3072 nt\n"
  "s_add_u32 m0, s57, 0\n"
  "s_nop 0\n"
  "buffer_load_dwordx4 v4, s[40:43], s78 offen offset:0 nt lds\n"
  "s_add_u32 m0, s57, 4294965248\n"
  "s_nop 0\n"
  "buffer_load_dwordx4 v4, s[40:43], s78 offen offset:3072 nt lds\n"
  "buffer_load_dwordx4 v[72:75], v4, s[40:43], s72 offen offset:128 nt\n"
  "buffer_load_dwordx4 v[76:79], v4, s[40:43], s72 offen offset:3200 nt\n"
  "buffer_load_dwordx4 v[96:99], v4, s[40:43], s77 offen offset:128 nt\n"
  "buffer_load_dwordx4 v[100:103], v4, s[40:43], s77 offen offset:3200 nt\n"
  "s_add_u32 m0, s57, 1920\n"
  "s_nop 0\n"
  "buffer_load_dwordx4 v4, s[40:43], s78 offen offset:128 nt lds\n"
  "s_add_u32 m0, s57, 4294967168\n"
  "s_nop 0\n"
  "buffer_load_dwordx4 v4, s[40:43], s78 offen offset:3200 nt lds\n"
  "buffer_load_dwordx4 v[80:83], v4, s[40:43], s72 offen offset:256 nt\n"
  "buffer_load_dwordx4 v[84:87], v4, s[40:43], s72 offen offset:3328 nt\n"
  "buffer_load_dwordx4 v[104:107], v4, s[40:43], s77 offen offset:256 nt\n"
  "buffer_load_dwordx4 v[108:111], v4, s[40:43], s77 offen offset:3328 nt\n"
  "s_add_u32 m0, s57, 3840\n"
  "s_nop 0\n"
  "buffer_load_dwordx4 v4, s[40:43], s78 offen offset:256 nt lds\n"
  "s_add_u32 m0, s57, 1792\n"
  "s_nop 0\n"
  "buffer_load_dwordx4 v4, s[40:43], s78 offen offset:3328 nt lds\n"
  "s_add_u32 m0, s49, 0\n"
  "s_nop 0\n"
  "buffer_load_dwordx4 v4, s[36:39], s48 offen offset:0 nt lds\n"
  "s_add_u32 m0, s49, 3968\n"
  "s_nop 0\n"
  "buffer_load_dwordx4 v4, s[36:39], s48 offen offset:128 nt lds\n"
  "s_add_u32 m0, s49, 7936\n"
  "s_nop 0\n"
  "buffer_load_dwordx4 v4, s[36:39], s48 offen offset:256 nt lds\n"
  "s_add_u32 s49, s49, s60\n"
  "s_add_u32 s48, s48, s60\n"
  ".LH_pCa:\n"
  "s_cmp_lt_u32 s59, 4\n"
  "s_cbranch_scc1 .LH_starta\n"
  "s_barrier\n"
  ".LH_starta:\n"
  "v_mbcnt_lo_u32_b32 v38, -1, 0\n"
  "v_mbcnt_hi_u32_b32 v38, -1, v38\n"
  "v_lshlrev_b32_e32 v38, 4, v38\n"
  "v_add_u32_e32 v38, s58, v38\n"
  "s_sub_u32 s62, s58, s57\n"
  "v_add_u32_e32 v39, s62, v3\n"
  "s_waitcnt vmcnt(0)\n"
  "s_add_u32 m0, s49, 0\n"
  "s_nop 0\n"
  "buffer_load_dwordx4 v4, s[36:39], s48 offen offset:0 nt lds\n"
  "s_add_u32 m0, s49, 3968\n"
  "s_nop 0\n"
  "buffer_load_dwordx4 v4, s[36:39], s48 offen offset:128 nt lds\n"
  "s_add_u32 m0, s49, 7936\n"
  "s_nop 0\n"
  "buffer_load_dwordx4 v4, s[36:39], s48 offen offset:256 nt lds\n"
  "s_add_u32 s49, s49, s60\n"
  "s_add_u32 s48, s48, s60\n"
  "s_add_u32 m0, s49, 0\n"
  "s_nop 0\n"
  "buffer_load_dwordx4 v4, s[36:39], s48 offen offset:0 nt lds\n"
  "s_add_u32 m0, s49, 3968\n"
  "s_nop 0\n"
  "buffer_load_dwordx4 v4, s[36:39], s48 offen offset:128 nt lds\n"
  "s_add_u32 m0, s49, 7936\n"
  "s_nop 0\n"
  "buffer_load_dwordx4 v4, s[36:39], s48 offen offset:256 nt lds\n"
  "s_add_u32 s49, s49, s60\n"
  "s_add_u32 s48, s48, s60\n"
  "ds_write_b128 v38, v[64:67]\n"
  "ds_write_b128 v38, v[68:71] offset:1024\n"
  "ds_read_b128 v[64:67], v39\n"
  "ds_read_b128 v[68:71], v39 offset:512\n"
  "ds_write_b128 v38, v[88:91]\n"
  "ds_write_b128 v38, v[92:95] offset:1024\n"
  "ds_read_b128 v[88:91], v39\n"
  "ds_read_b128 v[92:95], v39 offset:512\n"
  "ds_write_b128 v38, v[72:75]\n"
  "ds_write_b128 v38, v[76:79] offset:1024\n"
  "ds_read_b128 v[72:75], v39\n"
  "ds_read_b128 v[76:79], v39 offset:512\n"
  "ds_write_b128 v38, v[96:99]\n"
  "ds_write_b128 v38, v[100:103] offset:1024\n"
  "ds_read_b128 v[96:99], v39\n"
  "ds_read_b128 v[100:103], v39 offset:512\n"
  "ds_write_b128 v38, v[80:83]\n"
  "ds_write_b128 v38, v[84:87] offset:1024\n"
  "ds_read_b128 v[80:83], v39\n"
  "ds_read_b128 v[84:87], v39 offset:512\n"
  "ds_write_b128 v38, v[104:107]\n"
  "ds_write_b128 v38, v[108:111] offset:1024\n"
  "ds_read_b128 v[104:107], v39\n"
  "ds_read_b128 v[108:111], v39 offset:512\n"
  "s_barrier\n"
  "ds_read_b128 v[12:15], v0 offset:0\n"
  "ds_read_b128 v[16:19], v0 offset:512\n"
  "ds_read_b128 v[20:23], v3 offset:0\n"
  "ds_read_b128 v[24:27], v3 offset:512\n"
  "ds_read_b128 v[28:31], v0 offset:2048\n"
  "ds_read_b128 v[32:35], v0 offset:2560\n"
  "ds_read_b128 v[36:39], v0 offset:4096\n"
  "s_waitcnt lgkmcnt(3)\n"
  "v_mfma_f32_16x16x32_bf16 v[40:43], v[12:15], v[64:67], 0\n"
  "v_mfma_f32_16x16x32_bf16 v[40:43], v[16:19], v[68:71], v[40:43]\n"
  "v_mfma_f32_16x16x32_bf16 v[48:51], v[12:15], v[88:91], 0\n"
  "v_mfma_f32_16x16x32_bf16 v[48:51], v[16:19], v[92:95], v[48:51]\n"
  "v_mfma_f32_16x16x32_bf16 v[56:59], v[12:15], v[20:23], 0\n"
  "ds_read_b128 v[12:15], v0 offset:4608\n"
  "v_mfma_f32_16x16x32_bf16 v[56:59], v[16:19], v[24:27], v[56:59]\n"
  "ds_read_b128 v[16:19], v3 offset:2048\n"
  "s_waitcnt lgkmcnt(3)\n"
  "v_mfma_f32_16x16x32_bf16 v[60:63], v[28:31], v[20:23], 0\n"
  "ds_read_b128 v[20:23], v3 offset:2560\n"
  "v_mfma_f32_16x16x32_bf16 v[60:63], v[32:35], v[24:27], v[60:63]\n"
  "ds_read_b128 v[24:27], v0 offset:6144\n"
  "v_mfma_f32_16x16x32_bf16 v[52:55], v[28:31], v[88:91], 0\n"
  "v_mfma_f32_16x16x32_bf16 v[52:55], v[32:35], v[92:95], v[52:55]\n"
  "v_mfma_f32_16x16x32_bf16 v[44:47], v[28:31], v[64:67], 0\n"
  "ds_read_b128 v[28:31], v0 offset:6656\n"
  "v_mfma_f32_16x16x32_bf16 v[44:47], v[32:35], v[68:71], v[44:47]\n"
  "ds_read_b128 v[32:35], v0 offset:8192\n"
  "s_waitcnt lgkmcnt(3)\n"
  "v_mfma_f32_16x16x32_bf16 v[40:43], v[36:39], v[72:75], v[40:43]\n"
  "v_mfma_f32_16x16x32_bf16 v[40:43], v[12:15], v[76:79], v[40:43]\n"
  "v_mfma_f32_16x16x32_bf16 v[48:51], v[36:39], v[96:99], v[48:51]\n"
  "v_mfma_f32_16x16x32_bf16 v[48:51], v[12:15], v[100:103], v[48:51]\n"
  "v_mfma_f32_16x16x32_bf16 v[56:59], v[36:39], v[16:19], v[56:59]\n"
  "ds_read_b128 v[36:39], v0 offset:8704\n"
  "v_mfma_f32_16x16x32_bf16 v[56:59], v[12:15], v[20:23], v[56:59]\n"
  "ds_read_b128 v[12:15], v3 offset:4096\n"
  "s_waitcnt lgkmcnt(3)\n"
  "v_mfma_f32_16x16x32_bf16 v[60:63], v[24:27], v[16:19], v[60:63]\n"
  "ds_read_b128 v[16:19], v3 offset:4608\n"
  "v_mfma_f32_16x16x32_bf16 v[60:63], v[28:31], v[20:23], v[60:63]\n"
  "ds_read_b128 v[20:23], v0 offset:10240\n"
  "v_mfma_f32_16x16x32_bf16 v[52:55], v[24:27], v[96:99], v[52:55]\n"
  "v_mfma_f32_16x16x32_bf16 v[52:55], v[28:31], v[100:103], v[52:55]\n"
  "v_mfma_f32_16x16x32_bf16 v[44:47], v[24:27], v[72:75], v[44:47]\n"
  "ds_read_b128 v[24:27], v0 offset:10752\n"
  "v_mfma_f32_16x16x32_bf16 v[44:47], v[28:31], v[76:79], v[44:47]\n"
  "s_waitcnt lgkmcnt(2)\n"
  "v_mfma_f32_16x16x32_bf16 v[40:43], v[32:35], v[80:83], v[40:43]\n"
  "v_mfma_f32_16x16x32_bf16 v[40:43], v[36:39], v[84:87], v[40:43]\n"
  "v_mfma_f32_16x16x32_bf16 v[48:51], v[32:35], v[104:107], v[48:51]\n"
  "v_mfma_f32_16x16x32_bf16 v[48:51], v[36:39], v[108:111], v[48:51]\n"
  "v_mfma_f32_16x16x32_bf16 v[56:59], v[32:35], v[12:15], v[56:59]\n"
  "v_mfma_f32_16x16x32_bf16 v[56:59], v[36:39], v[16:19], v[56:59]\n"
  "s_waitcnt lgkmcnt(0)\n"
  "v_mfma_f32_16x16x32_bf16 v[60:63], v[20:23], v[12:15], v[60:63]\n"
  "v_mfma_f32_16x16x32_bf16 v[60:63], v[24:27], v[16:19], v[60:63]\n"
  "v_mfma_f32_16x16x32_bf16 v[52:55], v[20:23], v[104:107], v[52:55]\n"
  "v_mfma_f32_16x16x32_bf16 v[52:55], v[24:27], v[108:111], v[52:55]\n"
  "v_mfma_f32_16x16x32_bf16 v[44:47], v[20:23], v[80:83], v[44:47]\n"
  "v_mfma_f32_16x16x32_bf16 v[44:47], v[24:27], v[84:87], v[44:47]\n"
  "s_barrier\n"
  "s_add_u32 m0, s49, 0\n"
  "s_nop 0\n"
  "buffer_load_dwordx4 v4, s[36:39], s48 offen offset:0 nt lds\n"
  "s_add_u32 m0, s49, 3968\n"
  "s_nop 0\n"
  "buffer_load_dwordx4 v4, s[36:39], s48 offen offset:128 nt lds\n"
  "s_add_u32 m0, s49, 7936\n"
  "s_nop 0\n"
  "buffer_load_dwordx4 v4, s[36:39], s48 offen offset:256 nt lds\n"
  "s_and_b32 s62, s50, 3\n"
  "s_cmp_eq_u32 s62, 3\n"
  "s_cselect_b32 s54, s61, s60\n"
  "s_cmp_eq_u32 s62, 0\n"
  "s_cselect_b32 s55, s61, s60\n"
  "s_add_u32 s49, s49, s55\n"
  "s_add_u32 s48, s48, s60\n"
  "v_add_u32_e32 v0, s54, v0\n"
  "v_add_u32_e32 v1, s55, v1\n"
  "v_add_u32_e32 v2, s55, v2\n"
  "v_max3_f32 v12, v40, v41, v42\n"
  "v_max3_f32 v13, v48, v49, v50\n"
  "v_max3_f32 v14, v56, v57, v58\n"
  "v_max3_f32 v12, v12, v43, v44\n"
  "v_max3_f32 v13, v13, v51, v52\n"
  "v_max3_f32 v14, v14, v59, v60\n"
  "v_max3_f32 v12, v12, v45, v46\n"
  "v_max3_f32 v13, v13, v53, v54\n"
  "v_max3_f32 v14, v14, v61, v62\n"
  "v_max_f32_e32 v12, v12, v47\n"
  "v_max_f32_e32 v13, v13, v55\n"
  "v_max_f32_e32 v14, v14, v63\n"
  "v_mov_b32_e32 v15, v12\n"
  "v_mov_b32_e32 v16, v13\n"
  "v_mov_b32_e32 v17, v14\n"
  "s_nop 1\n"
  "v_permlane32_swap_b32_e32 v12, v15\n"
  "v_permlane32_swap_b32_e32 v13, v16\n"
  "v_permlane32_swap_b32_e32 v14, v17\n"
  "s_nop 1\n"
  "v_max_f32_e32 v12, v12, v15\n"
  "v_max_f32_e32 v13, v13, v16\n"
  "v_max_f32_e32 v14, v14, v17\n"
  "v_mov_b32_e32 v15, v12\n"
  "v_mov_b32_e32 v16, v13\n"
  "v_mov_b32_e32 v17, v14\n"
  "s_nop 1\n"
  "v_permlane16_swap_b32_e32 v12, v15\n"
  "v_permlane16_swap_b32_e32 v13, v16\n"
  "v_permlane16_swap_b32_e32 v14, v17\n"
  "s_nop 1\n"
  "v_max_f32_e32 v12, v12, v15\n"
  "v_max_f32_e32 v13, v13, v16\n"
  "v_max_f32_e32 v14, v14, v17\n"
  "v_mul_f32_e64 v6, -v12, s52\n"
  "v_mul_f32_e64 v8, -v13, s52\n"
  "v_mul_f32_e64 v10, -v14, s52\n"
  "v_fma_f32 v40, v40, s52, v6\n"
  "v_fma_f32 v41, v41, s52, v6\n"
  "v_fma_f32 v42, v42, s52, v6\n"
  "v_fma_f32 v43, v43, s52, v6\n"
  "v_fma_f32 v48, v48, s52, v8\n"
  "v_fma_f32 v49, v49, s52, v8\n"
  "v_fma_f32 v50, v50, s52, v8\n"
  "v_fma_f32 v51, v51, s52, v8\n"
  "v_fma_f32 v56, v56, s52, v10\n"
  "v_fma_f32 v57, v57, s52, v10\n"
  "v_fma_f32 v58, v58, s52, v10\n"
  "v_fma_f32 v59, v59, s52, v10\n"
  "v_fma_f32 v60, v60, s52, v10\n"
  "v_fma_f32 v61, v61, s52, v10\n"
  "v_fma_f32 v62, v62, s52, v10\n"
  "v_fma_f32 v63, v63, s52, v10\n"
  "v_fma_f32 v52, v52, s52, v8\n"
  "v_fma_f32 v53, v53, s52, v8\n"
  "v_fma_f32 v54, v54, s52, v8\n"
  "v_fma_f32 v55, v55, s52, v8\n"
  "v_fma_f32 v44, v44, s52, v6\n"
  "v_fma_f32 v45, v45, s52, v6\n"
  "v_fma_f32 v46, v46, s52, v6\n"
  "v_fma_f32 v47, v47, s52, v6\n"
  "v_exp_f32_e32 v40, v40\n"
  "v_exp_f32_e32 v41, v41\n"
  "v_exp_f32_e32 v42, v42\n"
  "v_exp_f32_e32 v43, v43\n"
  "v_exp_f32_e32 v44, v44\n"
  "v_exp_f32_e32 v45, v45\n"
  "v_exp_f32_e32 v46, v46\n"
  "v_exp_f32_e32 v47, v47\n"
  "v_exp_f32_e32 v48, v48\n"
  "v_exp_f32_e32 v49, v49\n"
  "v_exp_f32_e32 v50, v50\n"
  "v_exp_f32_e32 v51, v51\n"
  "v_exp_f32_e32 v52, v52\n"
  "v_exp_f32_e32 v53, v53\n"
  "v_exp_f32_e32 v54, v54\n"
  "v_exp_f32_e32 v55, v55\n"
  "v_exp_f32_e32 v56, v56\n"
  "v_exp_f32_e32 v57, v57\n"
  "v_exp_f32_e32 v58, v58\n"
  "v_exp_f32_e32 v59, v59\n"
  "v_exp_f32_e32 v60, v60\n"
  "v_exp_f32_e32 v61, v61\n"
  "v_exp_f32_e32 v62, v62\n"
  "v_exp_f32_e32 v63, v63\n"
  "v_add_f32_e32 v7, v40, v41\n"
  "v_add_f32_e32 v9, v48, v49\n"
  "v_add_f32_e32 v11, v56, v57\n"
  "v_add_f32_e32 v7, v7, v42\n"
  "v_add_f32_e32 v9, v9, v50\n"
  "v_add_f32_e32 v11, v11, v58\n"
  "v_add_f32_e32 v7, v7, v43\n"
  "v_add_f32_e32 v9, v9, v51\n"
  "v_add_f32_e32 v11, v11, v59\n"
  "v_add_f32_e32 v7, v7, v44\n"
  "v_add_f32_e32 v9, v9, v52\n"
  "v_add_f32_e32 v11, v11, v60\n"
  "v_add_f32_e32 v7, v7, v45\n"
  "v_add_f32_e32 v9, v9, v53\n"
  "v_add_f32_e32 v11, v11, v61\n"
  "v_add_f32_e32 v7, v7, v46\n"
  "v_add_f32_e32 v9, v9, v54\n"
  "v_add_f32_e32 v11, v11, v62\n"
  "v_add_f32_e32 v7, v7, v47\n"
  "v_add_f32_e32 v9, v9, v55\n"
  "v_add_f32_e32 v11, v11, v63\n"
  "v_cvt_pk_bf16_f32 v40, v40, v41\n"
  "v_cvt_pk_bf16_f32 v41, v42, v43\n"
  "v_cvt_pk_bf16_f32 v42, v44, v45\n"
  "v_cvt_pk_bf16_f32 v43, v46, v47\n"
  "v_cvt_pk_bf16_f32 v48, v48, v49\n"
  "v_cvt_pk_bf16_f32 v49, v50, v51\n"
  "v_cvt_pk_bf16_f32 v50, v52, v53\n"
  "v_cvt_pk_bf16_f32 v51, v54, v55\n"
  "v_cvt_pk_bf16_f32 v56, v56, v57\n"
  "v_cvt_pk_bf16_f32 v57, v58, v59\n"
  "v_cvt_pk_bf16_f32 v58, v60, v61\n"
  "v_cvt_pk_bf16_f32 v59, v62, v63\n"
  "ds_read_b64_tr_b16 v[12:13], v1 offset:0\n"
  "ds_read_b64_tr_b16 v[14:15], v1 offset:2048\n"
  "ds_read_b64_tr_b16 v[16:17], v2 offset:0\n"
  "ds_read_b64_tr_b16 v[18:19], v2 offset:2048\n"
  "ds_read_b64_tr_b16 v[20:21], v1 offset:512\n"
  "ds_read_b64_tr_b16 v[22:23], v1 offset:2560\n"
  "ds_read_b64_tr_b16 v[24:25], v2 offset:512\n"
  "ds_read_b64_tr_b16 v[26:27], v2 offset:2560\n"
  "s_waitcnt vmcnt(6)\n"
  "s_barrier\n"
  "s_waitcnt lgkmcnt(4)\n"
  "v_mfma_f32_16x16x32_bf16 v[112:115], v[12:15], v[40:43], 0\n"
  "ds_read_b64_tr_b16 v[28:29], v1 offset:4096\n"
  "v_mfma_f32_16x16x32_bf16 v[160:163], v[12:15], v[48:51], 0\n"
  "ds_read_b64_tr_b16 v[30:31], v1 offset:6144\n"
  "v_mfma_f32_16x16x32_bf16 v[208:211], v[12:15], v[56:59], 0\n"
  "ds_read_b64_tr_b16 v[32:33], v2 offset:4096\n"
  "v_mfma_f32_16x16x32_bf16 v[212:215], v[16:19], v[56:59], 0\n"
  "ds_read_b64_tr_b16 v[34:35], v2 offset:6144\n"
  "v_mfma_f32_16x16x32_bf16 v[164:167], v[16:19], v[48:51], 0\n"
  "ds_read_b64_tr_b16 v[36:37], v1 offset:4608\n"
  "v_mfma_f32_16x16x32_bf16 v[116:119], v[16:19], v[40:43], 0\n"
  "ds_read_b64_tr_b16 v[38:39], v1 offset:6656\n"
  "s_waitcnt lgkmcnt(6)\n"
  "v_mfma_f32_16x16x32_bf16 v[120:123], v[20:23], v[40:43], 0\n"
  "ds_read_b64_tr_b16 v[12:13], v2 offset:4608\n"
  "v_mfma_f32_16x16x32_bf16 v[168:171], v[20:23], v[48:51], 0\n"
  "ds_read_b64_tr_b16 v[14:15], v2 offset:6656\n"
  "v_mfma_f32_16x16x32_bf16 v[216:219], v[20:23], v[56:59], 0\n"
  "ds_read_b64_tr_b16 v[16:17], v1 offset:8192\n"
  "v_mfma_f32_16x16x32_bf16 v[220:223], v[24:27], v[56:59], 0\n"
  "ds_read_b64_tr_b16 v[18:19], v1 offset:10240\n"
  "v_mfma_f32_16x16x32_bf16 v[172:175], v[24:27], v[48:51], 0\n"
  "ds_read_b64_tr_b16 v[20:21], v2 offset:8192\n"
  "v_mfma_f32_16x16x32_bf16 v[124:127], v[24:27], v[40:43], 0\n"
  "ds_read_b64_tr_b16 v[22:23], v2 offset:10240\n"
  "s_waitcnt lgkmcnt(8)\n"
  "v_mfma_f32_16x16x32_bf16 v[128:131], v[28:31], v[40:43], 0\n"
  "ds_read_b64_tr_b16 v[24:25], v1 offset:8704\n"
  "v_mfma_f32_16x16x32_bf16 v[176:179], v[28:31], v[48:51], 0\n"
  "ds_read_b64_tr_b16 v[26:27], v1 offset:10752\n"
  "v_mfma_f32_16x16x32_bf16 v[224:227], v[28:31], v[56:59], 0\n"
  "ds_read_b64_tr_b16 v[28:29], v2 offset:8704\n"
  "v_mfma_f32_16x16x32_bf16 v[228:231], v[32:35], v[56:59], 0\n"
  "ds_read_b64_tr_b16 v[30:31], v2 offset:10752\n"
  "v_mfma_f32_16x16x32_bf16 v[180:183], v[32:35], v[48:51], 0\n"
  "v_mfma_f32_16x16x32_bf16 v[132:135], v[32:35], v[40:43], 0\n"
  "ds_read_b128 v[32:35], v0 offset:0\n"
  "s_waitcnt lgkmcnt(9)\n"
  "v_mfma_f32_16x16x32_bf16 v[136:139], v[36:39], v[40:43], 0\n"
  "v_mfma_f32_16x16x32_bf16 v[184:187], v[36:39], v[48:51], 0\n"
  "v_mfma_f32_16x16x32_bf16 v[232:235], v[36:39], v[56:59], 0\n"
  "ds_read_b128 v[36:39], v0 offset:512\n"
  "v_mfma_f32_16x16x32_bf16 v[236:239], v[12:15], v[56:59], 0\n"
  "v_mfma_f32_16x16x32_bf16 v[188:191], v[12:15], v[48:51], 0\n"
  "v_mfma_f32_16x16x32_bf16 v[140:143], v[12:15], v[40:43], 0\n"
  "ds_read_b128 v[12:15], v3 offset:0\n"
  "s_waitcnt lgkmcnt(7)\n"
  "v_mfma_f32_16x16x32_bf16 v[144:147], v[16:19], v[40:43], 0\n"
  "v_mfma_f32_16x16x32_bf16 v[192:195], v[16:19], v[48:51], 0\n"
  "v_mfma_f32_16x16x32_bf16 v[240:243], v[16:19], v[56:59], 0\n"
  "ds_read_b128 v[16:19], v3 offset:512\n"
  "v_mfma_f32_16x16x32_bf16 v[244:247], v[20:23], v[56:59], 0\n"
  "v_mfma_f32_16x16x32_bf16 v[196:199], v[20:23], v[48:51], 0\n"
  "v_mfma_f32_16x16x32_bf16 v[148:151], v[20:23], v[40:43], 0\n"
  "ds_read_b128 v[20:23], v0 offset:2048\n"
  "s_waitcnt lgkmcnt(5)\n"
  "v_mfma_f32_16x16x32_bf16 v[152:155], v[24:27], v[40:43], 0\n"
  "v_mfma_f32_16x16x32_bf16 v[200:203], v[24:27], v[48:51], 0\n"
  "v_mfma_f32_16x16x32_bf16 v[248:251], v[24:27], v[56:59], 0\n"
  "ds_read_b128 v[24:27], v0 offset:2560\n"
  "v_mfma_f32_16x16x32_bf16 v[252:255], v[28:31], v[56:59], 0\n"
  "v_mfma_f32_16x16x32_bf16 v[204:207], v[28:31], v[48:51], 0\n"
  "v_mfma_f32_16x16x32_bf16 v[156:159], v[28:31], v[40:43], 0\n"
  "ds_read_b128 v[28:31], v0 offset:4096\n"
  "s_waitcnt lgkmcnt(3)\n"
  "v_mfma_f32_16x16x32_bf16 v[40:43], v[32:35], v[64:67], 0\n"
  "v_mfma_f32_16x16x32_bf16 v[40:43], v[36:39], v[68:71], v[40:43]\n"
  "v_mfma_f32_16x16x32_bf16 v[48:51], v[32:35], v[88:91], 0\n"
  "v_mfma_f32_16x16x32_bf16 v[48:51], v[36:39], v[92:95], v[48:51]\n"
  "v_mfma_f32_16x16x32_bf16 v[56:59], v[32:35], v[12:15], 0\n"
  "ds_read_b128 v[32:35], v0 offset:4608\n"
  "v_mfma_f32_16x16x32_bf16 v[56:59], v[36:39], v[16:19], v[56:59]\n"
  "ds_read_b128 v[36:39], v3 offset:2048\n"
  "s_waitcnt lgkmcnt(3)\n"
  "v_mfma_f32_16x16x32_bf16 v[60:63], v[20:23], v[12:15], 0\n"
  "ds_read_b128 v[12:15], v3 offset:2560\n"
  "v_mfma_f32_16x16x32_bf16 v[60:63], v[24:27], v[16:19], v[60:63]\n"
  "ds_read_b128 v[16:19], v0 offset:6144\n"
  "v_mfma_f32_16x16x32_bf16 v[52:55], v[20:23], v[88:91], 0\n"
  "v_mfma_f32_16x16x32_bf16 v[52:55], v[24:27], v[92:95], v[52:55]\n"
  "v_mfma_f32_16x16x32_bf16 v[44:47], v[20:23], v[64:67], 0\n"
  "ds_read_b128 v[20:23], v0 offset:6656\n"
  "v_mfma_f32_16x16x32_bf16 v[44:47], v[24:27], v[68:71], v[44:47]\n"
  "ds_read_b128 v[24:27], v0 offset:8192\n"
  "s_waitcnt lgkmcnt(3)\n"
  "v_mfma_f32_16x16x32_bf16 v[40:43], v[28:31], v[72:75], v[40:43]\n"
  "v_mfma_f32_16x16x32_bf16 v[40:43], v[32:35], v[76:79], v[40:43]\n"
  "v_mfma_f32_16x16x32_bf16 v[48:51], v[28:31], v[96:99], v[48:51]\n"
  "v_mfma_f32_16x16x32_bf16 v[48:51], v[32:35], v[100:103], v[48:51]\n"
  "v_mfma_f32_16x16x32_bf16 v[56:59], v[28:31], v[36:39], v[56:59]\n"
  "ds_read_b128 v[28:31], v0 offset:8704\n"
  "v_mfma_f32_16x16x32_bf16 v[56:59], v[32:35], v[12:15], v[56:59]\n"
  "ds_read_b128 v[32:35], v3 offset:4096\n"
  "s_waitcnt lgkmcnt(3)\n"
  "v_mfma_f32_16x16x32_bf16 v[60:63], v[16:19], v[36:39], v[60:63]\n"
  "ds_read_b128 v[36:39], v3 offset:4608\n"
  "v_mfma_f32_16x16x32_bf16 v[60:63], v[20:23], v[12:15], v[60:63]\n"
  "ds_read_b128 v[12:15], v0 offset:10240\n"
  "v_mfma_f32_16x16x32_bf16 v[52:55], v[16:19], v[96:99], v[52:55]\n"
  "v_mfma_f32_16x16x32_bf16 v[52:55], v[20:23], v[100:103], v[52:55]\n"
  "v_mfma_f32_16x16x32_bf16 v[44:47], v[16:19], v[72:75], v[44:47]\n"
  "ds_read_b128 v[16:19], v0 offset:10752\n"
  "v_mfma_f32_16x16x32_bf16 v[44:47], v[20:23], v[76:79], v[44:47]\n"
  "s_waitcnt lgkmcnt(2)\n"
  "v_mfma_f32_16x16x32_bf16 v[40:43], v[24:27], v[80:83], v[40:43]\n"
  "v_mfma_f32_16x16x32_bf16 v[40:43], v[28:31], v[84:87], v[40:43]\n"
  "v_mfma_f32_16x16x32_bf16 v[48:51], v[24:27], v[104:107], v[48:51]\n"
  "v_mfma_f32_16x16x32_bf16 v[48:51], v[28:31], v[108:111], v[48:51]\n"
  "v_mfma_f32_16x16x32_bf16 v[56:59], v[24:27], v[32:35], v[56:59]\n"
  "v_mfma_f32_16x16x32_bf16 v[56:59], v[28:31], v[36:39], v[56:59]\n"
  "s_waitcnt lgkmcnt(0)\n"
  "v_mfma_f32_16x16x32_bf16 v[60:63], v[12:15], v[32:35], v[60:63]\n"
  "v_mfma_f32_16x16x32_bf16 v[60:63], v[16:19], v[36:39], v[60:63]\n"
  "v_mfma_f32_16x16x32_bf16 v[52:55], v[12:15], v[104:107], v[52:55]\n"
  "v_mfma_f32_16x16x32_bf16 v[52:55], v[16:19], v[108:111], v[52:55]\n"
  "v_mfma_f32_16x16x32_bf16 v[44:47], v[12:15], v[80:83], v[44:47]\n"
  "v_mfma_f32_16x16x32_bf16 v[44:47], v[16:19], v[84:87], v[44:47]\n"
  "s_mov_b32 s50, 1\n"
  ".LH_loopa:\n"
  "s_barrier\n"
  "s_nop 3\n"
  "v_fma_f32 v40, v40, s52, v6\n"
  "v_fma_f32 v41, v41, s52, v6\n"
  "s_add_u32 m0, s49, 0\n"
  "v_fma_f32 v42, v42, s52, v6\n"
  "v_fma_f32 v43, v43, s52, v6\n"
  "buffer_load_dwordx4 v4, s[36:39], s48 offen offset:0 nt lds\n"
  "s_add_u32 m0, s49, 3968\n"
  "v_fma_f32 v48, v48, s52, v8\n"
  "v_fma_f32 v49, v49, s52, v8\n"
  "buffer_load_dwordx4 v4, s[36:39], s48 offen offset:128 nt lds\n"
  "s_add_u32 m0, s49, 7936\n"
  "v_fma_f32 v50, v50, s52, v8\n"
  "v_fma_f32 v51, v51, s52, v8\n"
  "buffer_load_dwordx4 v4, s[36:39], s48 offen offset:256 nt lds\n"
  "s_and_b32 s62, s50, 3\n"
  "v_fma_f32 v56, v56, s52, v10\n"
  "v_fma_f32 v57, v57, s52, v10\n"
  "s_cmp_eq_u32 s62, 3\n"
  "s_cselect_b32 s54, s61, s60\n"
  "v_fma_f32 v58, v58, s52, v10\n"
  "v_fma_f32 v59, v59, s52, v10\n"
  "s_cmp_eq_u32 s62, 0\n"
  "s_cselect_b32 s55, s61, s60\n"
  "v_fma_f32 v60, v60, s52, v10\n"
  "v_fma_f32 v61, v61, s52, v10\n"
  "s_add_u32 s49, s49, s55\n"
  "s_add_u32 s48, s48, s60\n"
  "v_fma_f32 v62, v62, s52, v10\n"
  "v_fma_f32 v63, v63, s52, v10\n"
  "v_fma_f32 v52, v52, s52, v8\n"
  "v_fma_f32 v53, v53, s52, v8\n"
  "v_fma_f32 v54, v54, s52, v8\n"
  "v_fma_f32 v55, v55, s52, v8\n"
  "v_fma_f32 v44, v44, s52, v6\n"
  "v_fma_f32 v45, v45, s52, v6\n"
  "v_fma_f32 v46, v46, s52, v6\n"
  "v_fma_f32 v47, v47, s52, v6\n"
  "v_exp_f32_e32 v12, v40\n"
  "v_exp_f32_e32 v13, v41\n"
  "v_exp_f32_e32 v14, v42\n"
  "v_exp_f32_e32 v15, v43\n"
  "v_exp_f32_e32 v16, v44\n"
  "v_exp_f32_e32 v17, v45\n"
  "v_exp_f32_e32 v18, v46\n"
  "v_exp_f32_e32 v19, v47\n"
  "v_exp_f32_e32 v20, v48\n"
  "v_exp_f32_e32 v21, v49\n"
  "v_exp_f32_e32 v22, v50\n"
  "v_exp_f32_e32 v23, v51\n"
  "v_exp_f32_e32 v24, v52\n"
  "v_exp_f32_e32 v25, v53\n"
  "v_exp_f32_e32 v26, v54\n"
  "v_exp_f32_e32 v27, v55\n"
  "v_exp_f32_e32 v28, v56\n"
  "v_exp_f32_e32 v29, v57\n"
  "v_exp_f32_e32 v30, v58\n"
  "v_exp_f32_e32 v31, v59\n"
  "v_exp_f32_e32 v32, v60\n"
  "v_exp_f32_e32 v33, v61\n"
  "v_exp_f32_e32 v34, v62\n"
  "v_exp_f32_e32 v35, v63\n"
  "v_add_f32_e32 v36, v12, v13\n"
  "v_add_f32_e32 v37, v20, v21\n"
  "v_add_f32_e32 v38, v28, v29\n"
  "v_add_f32_e32 v36, v36, v14\n"
  "v_add_f32_e32 v37, v37, v22\n"
  "v_add_f32_e32 v38, v38, v30\n"
  "v_add_f32_e32 v36, v36, v15\n"
  "v_add_f32_e32 v37, v37, v23\n"
  "v_add_f32_e32 v38, v38, v31\n"
  "v_add_f32_e32 v36, v36, v16\n"
  "v_add_f32_e32 v37, v37, v24\n"
  "v_add_f32_e32 v38, v38, v32\n"
  "v_add_f32_e32 v36, v36, v17\n"
  "v_add_f32_e32 v37, v37, v25\n"
  "v_add_f32_e32 v38, v38, v33\n"
  "v_add_f32_e32 v36, v36, v18\n"
  "v_add_f32_e32 v37, v37, v26\n"
  "v_add_f32_e32 v38, v38, v34\n"
  "v_add_f32_e32 v36, v36, v19\n"
  "v_add_f32_e32 v37, v37, v27\n"
  "v_add_f32_e32 v38, v38, v35\n"
  "v_max3_f32 v39, v36, v37, v38\n"
  "v_cmp_lt_f32_e32 vcc, s53, v39\n"
  "v_add_u32_e32 v0, s54, v0\n"
  "v_add_u32_e32 v1, s55, v1\n"
  "v_add_u32_e32 v2, s55, v2\n"
  "s_cbranch_vccnz .LH_slowa\n"
  ".LH_slow_reta:\n"
  "v_add_f32_e32 v7, v7, v36\n"
  "v_add_f32_e32 v9, v9, v37\n"
  "v_add_f32_e32 v11, v11, v38\n"
  "v_cvt_pk_bf16_f32 v40, v12, v13\n"
  "v_cvt_pk_bf16_f32 v41, v14, v15\n"
  "v_cvt_pk_bf16_f32 v42, v16, v17\n"
  "v_cvt_pk_bf16_f32 v43, v18, v19\n"
  "v_cvt_pk_bf16_f32 v48, v20, v21\n"
  "v_cvt_pk_bf16_f32 v49, v22, v23\n"
  "v_cvt_pk_bf16_f32 v50, v24, v25\n"
  "v_cvt_pk_bf16_f32 v51, v26, v27\n"
  "v_cvt_pk_bf16_f32 v56, v28, v29\n"
  "v_cvt_pk_bf16_f32 v57, v30, v31\n"
  "v_cvt_pk_bf16_f32 v58, v32, v33\n"
  "v_cvt_pk_bf16_f32 v59, v34, v35\n"
  "ds_read_b64_tr_b16 v[12:13], v1 offset:0\n"
  "ds_read_b64_tr_b16 v[14:15], v1 offset:2048\n"
  "ds_read_b64_tr_b16 v[16:17], v2 offset:0\n"
  "ds_read_b64_tr_b16 v[18:19], v2 offset:2048\n"
  "ds_read_b64_tr_b16 v[20:21], v1 offset:512\n"
  "ds_read_b64_tr_b16 v[22:23], v1 offset:2560\n"
  "ds_read_b64_tr_b16 v[24:25], v2 offset:512\n"
  "ds_read_b64_tr_b16 v[26:27], v2 offset:2560\n"
  "s_bitcmp1_b32 s83, s50\n"
  "s_cbranch_scc1 .LH_speciala\n"
  "s_waitcnt vmcnt(6)\n"
  ".LH_mida:\n"
  "s_barrier\n"
  "s_waitcnt lgkmcnt(4)\n"
  "v_mfma_f32_16x16x32_bf16 v[112:115], v[12:15], v[40:43], v[112:115]\n"
  "ds_read_b64_tr_b16 v[28:29], v1 offset:4096\n"
  "v_mfma_f32_16x16x32_bf16 v[160:163], v[12:15], v[48:51], v[160:163]\n"
  "ds_read_b64_tr_b16 v[30:31], v1 offset:6144\n"
  "v_mfma_f32_16x16x32_bf16 v[208:211], v[12:15], v[56:59], v[208:211]\n"
  "ds_read_b64_tr_b16 v[32:33], v2 offset:4096\n"
  "v_mfma_f32_16x16x32_bf16 v[212:215], v[16:19], v[56:59], v[212:215]\n"
  "ds_read_b64_tr_b16 v[34:35], v2 offset:6144\n"
  "v_mfma_f32_16x16x32_bf16 v[164:167], v[16:19], v[48:51], v[164:167]\n"
  "ds_read_b64_tr_b16 v[36:37], v1 offset:4608\n"
  "v_mfma_f32_16x16x32_bf16 v[116:119], v[16:19], v[40:43], v[116:119]\n"
  "ds_read_b64_tr_b16 v[38:39], v1 offset:6656\n"
  "s_waitcnt lgkmcnt(6)\n"
  "v_mfma_f32_16x16x32_bf16 v[120:123], v[20:23], v[40:43], v[120:123]\n"
  "ds_read_b64_tr_b16 v[12:13], v2 offset:4608\n"
  "v_mfma_f32_16x16x32_bf16 v[168:171], v[20:23], v[48:51], v[168:171]\n"
  "ds_read_b64_tr_b16 v[14:15], v2 offset:6656\n"
  "v_mfma_f32_16x16x32_bf16 v[216:219], v[20:23], v[56:59], v[216:219]\n"
  "ds_read_b64_tr_b16 v[16:17], v1 offset:8192\n"
  "v_mfma_f32_16x16x32_bf16 v[220:223], v[24:27], v[56:59], v[220:223]\n"
  "ds_read_b64_tr_b16 v[18:19], v1 offset:10240\n"
  "v_mfma_f32_16x16x32_bf16 v[172:175], v[24:27], v[48:51], v[172:175]\n"
  "ds_read_b64_tr_b16 v[20:21], v2 offset:8192\n"
  "v_mfma_f32_16x16x32_bf16 v[124:127], v[24:27], v[40:43], v[124:127]\n"
  "ds_read_b64_tr_b16 v[22:23], v2 offset:10240\n"
  "s_waitcnt lgkmcnt(8)\n"
  "v_mfma_f32_16x16x32_bf16 v[128:131], v[28:31], v[40:43], v[128:131]\n"
  "ds_read_b64_tr_b16 v[24:25], v1 offset:8704\n"
  "v_mfma_f32_16x16x32_bf16 v[176:179], v[28:31], v[48:51], v[176:179]\n"
  "ds_read_b64_tr_b16 v[26:27], v1 offset:10752\n"
  "v_mfma_f32_16x16x32_bf16 v[224:227], v[28:31], v[56:59], v[224:227]\n"
  "ds_read_b64_tr_b16 v[28:29], v2 offset:8704\n"
  "v_mfma_f32_16x16x32_bf16 v[228:231], v[32:35], v[56:59], v[228:231]\n"
  "ds_read_b64_tr_b16 v[30:31], v2 offset:10752\n"
  "v_mfma_f32_16x16x32_bf16 v[180:183], v[32:35], v[48:51], v[180:183]\n"
  "v_mfma_f32_16x16x32_bf16 v[132:135], v[32:35], v[40:43], v[132:135]\n"
  "ds_read_b128 v[32:35], v0 offset:0\n"
  "s_waitcnt lgkmcnt(9)\n"
  "v_mfma_f32_16x16x32_bf16 v[136:139], v[36:39], v[40:43], v[136:139]\n"
  "v_mfma_f32_16x16x32_bf16 v[184:187], v[36:39], v[48:51], v[184:187]\n"
  "v_mfma_f32_16x16x32_bf16 v[232:235], v[36:39], v[56:59], v[232:235]\n"
  "ds_read_b128 v[36:39], v0 offset:512\n"
  "v_mfma_f32_16x16x32_bf16 v[236:239], v[12:15], v[56:59], v[236:239]\n"
  "v_mfma_f32_16x16x32_bf16 v[188:191], v[12:15], v[48:51], v[188:191]\n"
  "v_mfma_f32_16x16x32_bf16 v[140:143], v[12:15], v[40:43], v[140:143]\n"
  "ds_read_b128 v[12:15], v3 offset:0\n"
  "s_waitcnt lgkmcnt(7)\n"
  "v_mfma_f32_16x16x32_bf16 v[144:147], v[16:19], v[40:43], v[144:147]\n"
  "v_mfma_f32_16x16x32_bf16 v[192:195], v[16:19], v[48:51], v[192:195]\n"
  "v_mfma_f32_16x16x32_bf16 v[240:243], v[16:19], v[56:59], v[240:243]\n"
  "ds_read_b128 v[16:19], v3 offset:512\n"
  "v_mfma_f32_16x16x32_bf16 v[244:247], v[20:23], v[56:59], v[244:247]\n"
  "v_mfma_f32_16x16x32_bf16 v[196:199], v[20:23], v[48:51], v[196:199]\n"
  "v_mfma_f32_16x16x32_bf16 v[148:151], v[20:23], v[40:43], v[148:151]\n"
  "ds_read_b128 v[20:23], v0 offset:2048\n"
  "s_waitcnt lgkmcnt(5)\n"
  "v_mfma_f32_16x16x32_bf16 v[152:155], v[24:27], v[40:43], v[152:155]\n"
  "v_mfma_f32_16x16x32_bf16 v[200:203], v[24:27], v[48:51], v[200:203]\n"
  "v_mfma_f32_16x16x32_bf16 v[248:251], v[24:27], v[56:59], v[248:251]\n"
  "ds_read_b128 v[24:27], v0 offset:2560\n"
  "v_mfma_f32_16x16x32_bf16 v[252:255], v[28:31], v[56:59], v[252:255]\n"
  "v_mfma_f32_16x16x32_bf16 v[204:207], v[28:31], v[48:51], v[204:207]\n"
  "v_mfma_f32_16x16x32_bf16 v[156:159], v[28:31], v[40:43], v[156:159]\n"
  "ds_read_b128 v[28:31], v0 offset:4096\n"
  "s_waitcnt lgkmcnt(3)\n"
  "v_mfma_f32_16x16x32_bf16 v[40:43], v[32:35], v[64:67], 0\n"
  "v_mfma_f32_16x16x32_bf16 v[40:43], v[36:39], v[68:71], v[40:43]\n"
  "v_mfma_f32_16x16x32_bf16 v[48:51], v[32:35], v[88:91], 0\n"
  "v_mfma_f32_16x16x32_bf16 v[48:51], v[36:39], v[92:95], v[48:51]\n"
  "v_mfma_f32_16x16x32_bf16 v[56:59], v[32:35], v[12:15], 0\n"
  "ds_read_b128 v[32:35], v0 offset:4608\n"
  "v_mfma_f32_16x16x32_bf16 v[56:59], v[36:39], v[16:19], v[56:59]\n"
  "ds_read_b128 v[36:39], v3 offset:2048\n"
  "s_waitcnt lgkmcnt(3)\n"
  "v_mfma_f32_16x16x32_bf16 v[60:63], v[20:23], v[12:15], 0\n"
  "ds_read_b128 v[12:15], v3 offset:2560\n"
  "v_mfma_f32_16x16x32_bf16 v[60:63], v[24:27], v[16:19], v[60:63]\n"
  "ds_read_b128 v[16:19], v0 offset:6144\n"
  "v_mfma_f32_16x16x32_bf16 v[52:55], v[20:23], v[88:91], 0\n"
  "v_mfma_f32_16x16x32_bf16 v[52:55], v[24:27], v[92:95], v[52:55]\n"
  "v_mfma_f32_16x16x32_bf16 v[44:47], v[20:23], v[64:67], 0\n"
  "ds_read_b128 v[20:23], v0 offset:6656\n"
  "v_mfma_f32_16x16x32_bf16 v[44:47], v[24:27], v[68:71], v[44:47]\n"
  "ds_read_b128 v[24:27], v0 offset:8192\n"
  "s_waitcnt lgkmcnt(3)\n"
  "v_mfma_f32_16x16x32_bf16 v[40:43], v[28:31], v[72:75], v[40:43]\n"
  "v_mfma_f32_16x16x32_bf16 v[40:43], v[32:35], v[76:79], v[40:43]\n"
  "v_mfma_f32_16x16x32_bf16 v[48:51], v[28:31], v[96:99], v[48:51]\n"
  "v_mfma_f32_16x16x32_bf16 v[48:51], v[32:35], v[100:103], v[48:51]\n"
  "v_mfma_f32_16x16x32_bf16 v[56:59], v[28:31], v[36:39], v[56:59]\n"
  "ds_read_b128 v[28:31], v0 offset:8704\n"
  "v_mfma_f32_16x16x32_bf16 v[56:59], v[32:35], v[12:15], v[56:59]\n"
  "ds_read_b128 v[32:35], v3 offset:4096\n"
  "s_waitcnt lgkmcnt(3)\n"
  "v_mfma_f32_16x16x32_bf16 v[60:63], v[16:19], v[36:39], v[60:63]\n"
  "ds_read_b128 v[36:39], v3 offset:4608\n"
  "v_mfma_f32_16x16x32_bf16 v[60:63], v[20:23], v[12:15], v[60:63]\n"
  "ds_read_b128 v[12:15], v0 offset:10240\n"
  "v_mfma_f32_16x16x32_bf16 v[52:55], v[16:19], v[96:99], v[52:55]\n"
  "v_mfma_f32_16x16x32_bf16 v[52:55], v[20:23], v[100:103], v[52:55]\n"
  "v_mfma_f32_16x16x32_bf16 v[44:47], v[16:19], v[72:75], v[44:47]\n"
  "ds_read_b128 v[16:19], v0 offset:10752\n"
  "v_mfma_f32_16x16x32_bf16 v[44:47], v[20:23], v[76:79], v[44:47]\n"
  "s_waitcnt lgkmcnt(2)\n"
  "v_mfma_f32_16x16x32_bf16 v[40:43], v[24:27], v[80:83], v[40:43]\n"
  "v_mfma_f32_16x16x32_bf16 v[40:43], v[28:31], v[84:87], v[40:43]\n"
  "v_mfma_f32_16x16x32_bf16 v[48:51], v[24:27], v[104:107], v[48:51]\n"
  "v_mfma_f32_16x16x32_bf16 v[48:51], v[28:31], v[108:111], v[48:51]\n"
  "v_mfma_f32_16x16x32_bf16 v[56:59], v[24:27], v[32:35], v[56:59]\n"
  "v_mfma_f32_16x16x32_bf16 v[56:59], v[28:31], v[36:39], v[56:59]\n"
  "s_waitcnt lgkmcnt(0)\n"
  "v_mfma_f32_16x16x32_bf16 v[60:63], v[12:15], v[32:35], v[60:63]\n"
  "v_mfma_f32_16x16x32_bf16 v[60:63], v[16:19], v[36:39], v[60:63]\n"
  "v_mfma_f32_16x16x32_bf16 v[52:55], v[12:15], v[104:107], v[52:55]\n"
  "v_mfma_f32_16x16x32_bf16 v[52:55], v[16:19], v[108:111], v[52:55]\n"
  "v_mfma_f32_16x16x32_bf16 v[44:47], v[12:15], v[80:83], v[44:47]\n"
  "v_mfma_f32_16x16x32_bf16 v[44:47], v[16:19], v[84:87], v[44:47]\n"
  "s_add_u32 s50, s50, 1\n"
  "s_bitcmp0_b32 s79, s50\n"
  "s_cbranch_scc1 .LH_loopa\n"
  "s_and_b32 s62, s50, 15\n"
  "s_cmp_eq_u32 s62, 15\n"
  "s_cbranch_scc1 .LH_t15a\n"
  "s_add_u32 m0, s66, 159744\n"
  "s_nop 0\n"
  "buffer_load_dword v5, s[40:43], s73 offen lds\n"
  "s_add_u32 s73, s73, 65536\n"
  "s_branch .LH_loopa\n"
  ".LH_t15a:\n"
  "s_barrier\n"
  "s_and_b32 s62, s50, 3\n"
  "s_cmp_eq_u32 s62, 3\n"
  "s_cselect_b32 s54, s61, s60\n"
  "s_cmp_eq_u32 s62, 0\n"
  "s_cselect_b32 s55, s61, s60\n"
  "v_add_u32_e32 v0, s54, v0\n"
  "v_add_u32_e32 v1, s55, v1\n"
  "v_add_u32_e32 v2, s55, v2\n"
  "s_add_u32 s72, s72, 141312\n"
  "s_add_u32 s77, s77, 141312\n"
  "s_add_u32 s78, s78, 141312\n"
  "v_fma_f32 v40, v40, s52, v6\n"
  "v_fma_f32 v41, v41, s52, v6\n"
  "buffer_load_dwordx4 v[64:67], v4, s[40:43], s72 offen offset:0 nt\n"
  "v_fma_f32 v42, v42, s52, v6\n"
  "v_fma_f32 v43, v43, s52, v6\n"
  "v_fma_f32 v48, v48, s52, v8\n"
  "v_fma_f32 v49, v49, s52, v8\n"
  "v_fma_f32 v50, v50, s52, v8\n"
  "v_fma_f32 v51, v51, s52, v8\n"
  "buffer_load_dwordx4 v[68:71], v4, s[40:43], s72 offen offset:3072 nt\n"
  "v_fma_f32 v56, v56, s52, v10\n"
  "v_fma_f32 v57, v57, s52, v10\n"
  "v_fma_f32 v58, v58, s52, v10\n"
  "v_fma_f32 v59, v59, s52, v10\n"
  "v_fma_f32 v60, v60, s52, v10\n"
  "buffer_load_dwordx4 v[88:91], v4, s[40:43], s77 offen offset:0 nt\n"
  "v_fma_f32 v61, v61, s52, v10\n"
  "v_fma_f32 v62, v62, s52, v10\n"
  "v_fma_f32 v63, v63, s52, v10\n"
  "v_fma_f32 v52, v52, s52, v8\n"
  "v_fma_f32 v53, v53, s52, v8\n"
  "buffer_load_dwordx4 v[92:95], v4, s[40:43], s77 offen offset:3072 nt\n"
  "v_fma_f32 v54, v54, s52, v8\n"
  "v_fma_f32 v55, v55, s52, v8\n"
  "v_fma_f32 v44, v44, s52, v6\n"
  "v_fma_f32 v45, v45, s52, v6\n"
  "v_fma_f32 v46, v46, s52, v6\n"
  "v_fma_f32 v47, v47, s52, v6\n"
  "s_add_u32 m0, s57, 0\n"
  "s_nop 0\n"
  "buffer_load_dwordx4 v4, s[40:43], s78 offen offset:0 nt lds\n"
  "v_exp_f32_e32 v12, v40\n"
  "v_exp_f32_e32 v13, v41\n"
  "v_exp_f32_e32 v14, v42\n"
  "v_exp_f32_e32 v15, v43\n"
  "v_exp_f32_e32 v16, v44\n"
  "s_add_u32 m0, s57, 4294965248\n"
  "s_nop 0\n"
  "buffer_load_dwordx4 v4, s[40:43], s78 offen offset:3072 nt lds\n"
  "v_exp_f32_e32 v17, v45\n"
  "v_exp_f32_e32 v18, v46\n"
  "v_exp_f32_e32 v19, v47\n"
  "v_exp_f32_e32 v20, v48\n"
  "v_exp_f32_e32 v21, v49\n"
  "buffer_load_dwordx4 v[72:75], v4, s[40:43], s72 offen offset:128 nt\n"
  "v_exp_f32_e32 v22, v50\n"
  "v_exp_f32_e32 v23, v51\n"
  "v_exp_f32_e32 v24, v52\n"
  "v_exp_f32_e32 v25, v53\n"
  "v_exp_f32_e32 v26, v54\n"
  "v_exp_f32_e32 v27, v55\n"
  "buffer_load_dwordx4 v[76:79], v4, s[40:43], s72 offen offset:3200 nt\n"
  "v_exp_f32_e32 v28, v56\n"
  "v_exp_f32_e32 v29, v57\n"
  "v_exp_f32_e32 v30, v58\n"
  "v_exp_f32_e32 v31, v59\n"
  "v_exp_f32_e32 v32, v60\n"
  "buffer_load_dwordx4 v[96:99], v4, s[40:43], s77 offen offset:128 nt\n"
  "v_exp_f32_e32 v33, v61\n"
  "v_exp_f32_e32 v34, v62\n"
  "v_exp_f32_e32 v35, v63\n"
  "v_add_f32_e32 v36, v12, v13\n"
  "v_add_f32_e32 v37, v20, v21\n"
  "buffer_load_dwordx4 v[100:103], v4, s[40:43], s77 offen offset:3200 nt\n"
  "v_add_f32_e32 v38, v28, v29\n"
  "v_add_f32_e32 v36, v36, v14\n"
  "v_add_f32_e32 v37, v37, v22\n"
  "v_add_f32_e32 v38, v38, v30\n"
  "v_add_f32_e32 v36, v36, v15\n"
  "v_add_f32_e32 v37, v37, v23\n"
  "s_add_u32 m0, s57, 1920\n"
  "s_nop 0\n"
  "buffer_load_dwordx4 v4, s[40:43], s78 offen offset:128 nt lds\n"
  "v_add_f32_e32 v38, v38, v31\n"
  "v_add_f32_e32 v36, v36, v16\n"
  "v_add_f32_e32 v37, v37, v24\n"
  "v_add_f32_e32 v38, v38, v32\n"
  "v_add_f32_e32 v36, v36, v17\n"
  "s_add_u32 m0, s57, 4294967168\n"
  "s_nop 0\n"
  "buffer_load_dwordx4 v4, s[40:43], s78 offen offset:3200 nt lds\n"
  "v_add_f32_e32 v37, v37, v25\n"
  "v_add_f32_e32 v38, v38, v33\n"
  "v_add_f32_e32 v36, v36, v18\n"
  "v_add_f32_e32 v37, v37, v26\n"
  "v_add_f32_e32 v38, v38, v34\n"
  "buffer_load_dwordx4 v[80:83], v4, s[40:43], s72 offen offset:256 nt\n"
  "v_add_f32_e32 v36, v36, v19\n"
  "v_add_f32_e32 v37, v37, v27\n"
  "v_add_f32_e32 v38, v38, v35\n"
  "v_max3_f32 v39, v36, v37, v38\n"
  "v_cmp_lt_f32_e32 vcc, s53, v39\n"
  "s_cbranch_vccnz .LH_slowa\n"
  ".LH_slow_ret15a:\n"
  "buffer_load_dwordx4 v[84:87], v4, s[40:43], s72 offen offset:3328 nt\n"
  "v_add_f32_e32 v7, v7, v36\n"
  "v_add_f32_e32 v9, v9, v37\n"
  "v_add_f32_e32 v11, v11, v38\n"
  "v_cvt_pk_bf16_f32 v40, v12, v13\n"
  "buffer_load_dwordx4 v[104:107], v4, s[40:43], s77 offen offset:256 nt\n"
  "v_cvt_pk_bf16_f32 v41, v14, v15\n"
  "v_cvt_pk_bf16_f32 v42, v16, v17\n"
  "v_cvt_pk_bf16_f32 v43, v18, v19\n"
  "v_cvt_pk_bf16_f32 v48, v20, v21\n"
  "v_cvt_pk_bf16_f32 v49, v22, v23\n"
  "buffer_load_dwordx4 v[108:111], v4, s[40:43], s77 offen offset:3328 nt\n"
  "v_cvt_pk_bf16_f32 v50, v24, v25\n"
  "v_cvt_pk_bf16_f32 v51, v26, v27\n"
  "v_cvt_pk_bf16_f32 v56, v28, v29\n"
  "v_cvt_pk_bf16_f32 v57, v30, v31\n"
  "v_cvt_pk_bf16_f32 v58, v32, v33\n"
  "v_cvt_pk_bf16_f32 v59, v34, v35\n"
  "s_add_u32 m0, s57, 3840\n"
  "s_nop 0\n"
  "buffer_load_dwordx4 v4, s[40:43], s78 offen offset:256 nt lds\n"
  "ds_read_b64_tr_b16 v[12:13], v1 offset:0\n"
  "ds_read_b64_tr_b16 v[14:15], v1 offset:2048\n"
  "ds_read_b64_tr_b16 v[16:17], v2 offset:0\n"
  "ds_read_b64_tr_b16 v[18:19], v2 offset:2048\n"
  "ds_read_b64_tr_b16 v[20:21], v1 offset:512\n"
  "s_add_u32 m0, s57, 1792\n"
  "s_nop 0\n"
  "buffer_load_dwordx4 v4, s[40:43], s78 offen offset:3328 nt lds\n"
  "ds_read_b64_tr_b16 v[22:23], v1 offset:2560\n"
  "ds_read_b64_tr_b16 v[24:25], v2 offset:512\n"
  "ds_read_b64_tr_b16 v[26:27], v2 offset:2560\n"
  "v_mov_b32_e32 v44, v7\n"
  "v_mov_b32_e32 v45, v9\n"
  "v_mov_b32_e32 v46, v11\n"
  "s_nop 1\n"
  "v_permlane32_swap_b32_e32 v7, v44\n"
  "v_permlane32_swap_b32_e32 v9, v45\n"
  "s_add_u32 m0, s49, 0\n"
  "s_nop 0\n"
  "buffer_load_dwordx4 v4, s[36:39], s48 offen offset:0 nt lds\n"
  "v_permlane32_swap_b32_e32 v11, v46\n"
  "s_nop 1\n"
  "v_add_f32_e32 v7, v7, v44\n"
  "v_add_f32_e32 v9, v9, v45\n"
  "v_add_f32_e32 v11, v11, v46\n"
  "v_mov_b32_e32 v44, v7\n"
  "v_mov_b32_e32 v45, v9\n"
  "v_mov_b32_e32 v46, v11\n"
  "s_nop 1\n"
  "v_permlane16_swap_b32_e32 v7, v44\n"
  "v_permlane16_swap_b32_e32 v9, v45\n"
  "v_permlane16_swap_b32_e32 v11, v46\n"
  "s_nop 1\n"
  "s_add_u32 m0, s49, 3968\n"
  "s_nop 0\n"
  "buffer_load_dwordx4 v4, s[36:39], s48 offen offset:128 nt lds\n"
  "v_add_f32_e32 v7, v7, v44\n"
  "v_add_f32_e32 v9, v9, v45\n"
  "v_add_f32_e32 v11, v11, v46\n"
  "v_rcp_f32_e32 v32, v7\n"
  "v_rcp_f32_e32 v34, v9\n"
  "v_rcp_f32_e32 v36, v11\n"
  "v_mbcnt_lo_u32_b32 v52, -1, 0\n"
  "v_mbcnt_hi_u32_b32 v52, -1, v52\n"
  "v_and_b32_e32 v53, 15, v52\n"
  "v_mul_u32_u24_e32 v33, 136, v53\n"
  "v_lshrrev_b32_e32 v53, 4, v52\n"
  "v_lshl_add_u32 v33, v53, 3, v33\n"
  "v_add_u32_e32 v33, s58, v33\n"
  "s_add_u32 m0, s49, 7936\n"
  "s_nop 0\n"
  "buffer_load_dwordx4 v4, s[36:39], s48 offen offset:256 nt lds\n"
  "v_lshrrev_b32_e32 v53, 3, v52\n"
  "v_and_b32_e32 v52, 7, v52\n"
  "v_lshlrev_b32_e32 v52, 4, v52\n"
  "v_mad_u32_u24 v35, v53, s65, v52\n"
  "v_add_u32_e32 v35, s58, v35\n"
  "v_mul_u32_u24_e32 v53, 384, v53\n"
  "v_add_u32_e32 v37, v53, v52\n"
  "s_add_u32 s49, s49, s55\n"
  "s_add_u32 s48, s48, s60\n"
  "s_cmp_eq_u32 s50, 31\n"
  "s_cbranch_scc1 .LH_fina\n"
  "s_waitcnt vmcnt(24)\n"
  "s_barrier\n"
  "s_waitcnt lgkmcnt(4)\n"
  "v_mfma_f32_16x16x32_bf16 v[112:115], v[12:15], v[40:43], v[112:115]\n"
  "ds_read_b64_tr_b16 v[28:29], v1 offset:4096\n"
  "v_mfma_f32_16x16x32_bf16 v[160:163], v[12:15], v[48:51], v[160:163]\n"
  "ds_read_b64_tr_b16 v[30:31], v1 offset:6144\n"
  "v_mfma_f32_16x16x32_bf16 v[208:211], v[12:15], v[56:59], v[208:211]\n"
  "ds_read_b64_tr_b16 v[12:13], v2 offset:4096\n"
  "v_mfma_f32_16x16x32_bf16 v[212:215], v[16:19], v[56:59], v[212:215]\n"
  "ds_read_b64_tr_b16 v[14:15], v2 offset:6144\n"
  "v_mfma_f32_16x16x32_bf16 v[164:167], v[16:19], v[48:51], v[164:167]\n"
  "v_mfma_f32_16x16x32_bf16 v[116:119], v[16:19], v[40:43], v[116:119]\n"
  "ds_read_b64_tr_b16 v[16:17], v1 offset:4608\n"
  "v_mul_f32_e32 v112, v32, v112\n"
  "s_waitcnt lgkmcnt(5)\n"
  "v_mfma_f32_16x16x32_bf16 v[120:123], v[20:23], v[40:43], v[120:123]\n"
  "ds_read_b64_tr_b16 v[18:19], v1 offset:6656\n"
  "v_mul_f32_e32 v113, v32, v113\n"
  "v_mfma_f32_16x16x32_bf16 v[168:171], v[20:23], v[48:51], v[168:171]\n"
  "v_mul_f32_e32 v114, v32, v114\n"
  "v_mul_f32_e32 v115, v32, v115\n"
  "v_mfma_f32_16x16x32_bf16 v[216:219], v[20:23], v[56:59], v[216:219]\n"
  "ds_read_b64_tr_b16 v[20:21], v2 offset:4608\n"
  "v_cvt_pk_bf16_f32 v112, v112, v113\n"
  "v_mfma_f32_16x16x32_bf16 v[220:223], v[24:27], v[56:59], v[220:223]\n"
  "ds_read_b64_tr_b16 v[22:23], v2 offset:6656\n"
  "v_cvt_pk_bf16_f32 v113, v114, v115\n"
  "v_mfma_f32_16x16x32_bf16 v[172:175], v[24:27], v[48:51], v[172:175]\n"
  "ds_write_b64 v33, v[112:113] offset:0\n"
  "v_mul_f32_e32 v116, v32, v116\n"
  "v_mfma_f32_16x16x32_bf16 v[124:127], v[24:27], v[40:43], v[124:127]\n"
  "ds_read_b64_tr_b16 v[24:25], v1 offset:8192\n"
  "v_mul_f32_e32 v117, v32, v117\n"
  "s_waitcnt lgkmcnt(6)\n"
  "v_mfma_f32_16x16x32_bf16 v[128:131], v[28:31], v[40:43], v[128:131]\n"
  "ds_read_b64_tr_b16 v[26:27], v1 offset:10240\n"
  "v_mul_f32_e32 v118, v32, v118\n"
  "v_mfma_f32_16x16x32_bf16 v[176:179], v[28:31], v[48:51], v[176:179]\n"
  "v_mul_f32_e32 v119, v32, v119\n"
  "v_cvt_pk_bf16_f32 v116, v116, v117\n"
  "v_mfma_f32_16x16x32_bf16 v[224:227], v[28:31], v[56:59], v[224:227]\n"
  "ds_read_b64_tr_b16 v[28:29], v2 offset:8192\n"
  "v_cvt_pk_bf16_f32 v117, v118, v119\n"
  "v_mfma_f32_16x16x32_bf16 v[228:231], v[12:15], v[56:59], v[228:231]\n"
  "ds_read_b64_tr_b16 v[30:31], v2 offset:10240\n"
  "ds_write_b64 v33, v[116:117] offset:32\n"
  "v_mfma_f32_16x16x32_bf16 v[180:183], v[12:15], v[48:51], v[180:183]\n"
  "v_mul_f32_e32 v120, v32, v120\n"
  "v_mul_f32_e32 v121, v32, v121\n"
  "v_mfma_f32_16x16x32_bf16 v[132:135], v[12:15], v[40:43], v[132:135]\n"
  "ds_read_b64_tr_b16 v[12:13], v1 offset:8704\n"
  "v_mul_f32_e32 v122, v32, v122\n"
  "s_waitcnt lgkmcnt(7)\n"
  "v_mfma_f32_16x16x32_bf16 v[136:139], v[16:19], v[40:43], v[136:139]\n"
  "ds_read_b64_tr_b16 v[14:15], v1 offset:10752\n"
  "v_mul_f32_e32 v123, v32, v123\n"
  "v_mfma_f32_16x16x32_bf16 v[184:187], v[16:19], v[48:51], v[184:187]\n"
  "v_cvt_pk_bf16_f32 v120, v120, v121\n"
  "v_cvt_pk_bf16_f32 v121, v122, v123\n"
  "v_mfma_f32_16x16x32_bf16 v[232:235], v[16:19], v[56:59], v[232:235]\n"
  "ds_read_b64_tr_b16 v[16:17], v2 offset:8704\n"
  "ds_write_b64 v33, v[120:121] offset:64\n"
  "v_mfma_f32_16x16x32_bf16 v[236:239], v[20:23], v[56:59], v[236:239]\n"
  "ds_read_b64_tr_b16 v[18:19], v2 offset:10752\n"
  "v_mul_f32_e32 v124, v32, v124\n"
  "v_mfma_f32_16x16x32_bf16 v[188:191], v[20:23], v[48:51], v[188:191]\n"
  "v_mul_f32_e32 v125, v32, v125\n"
  "v_mul_f32_e32 v126, v32, v126\n"
  "v_mfma_f32_16x16x32_bf16 v[140:143], v[20:23], v[40:43], v[140:143]\n"
  "ds_read_b128 v[20:23], v0 offset:0\n"
  "v_mul_f32_e32 v127, v32, v127\n"
  "s_waitcnt lgkmcnt(7)\n"
  "v_mfma_f32_16x16x32_bf16 v[144:147], v[24:27], v[40:43], v[144:147]\n"
  "v_cvt_pk_bf16_f32 v124, v124, v125\n"
  "v_cvt_pk_bf16_f32 v125, v126, v127\n"
  "v_mfma_f32_16x16x32_bf16 v[192:195], v[24:27], v[48:51], v[192:195]\n"
  "ds_write_b64 v33, v[124:125] offset:96\n"
  "ds_read2_b64 v[112:115], v35 offset0:0 offset1:1\n"
  "v_mfma_f32_16x16x32_bf16 v[240:243], v[24:27], v[56:59], v[240:243]\n"
  "ds_read2_b64 v[116:119], v35 offset0:136 offset1:137\n"
  "v_mfma_f32_16x16x32_bf16 v[244:247], v[28:31], v[56:59], v[244:247]\n"
  "v_mfma_f32_16x16x32_bf16 v[196:199], v[28:31], v[48:51], v[196:199]\n"
  "v_mfma_f32_16x16x32_bf16 v[148:151], v[28:31], v[40:43], v[148:151]\n"
  "s_waitcnt lgkmcnt(4)\n"
  "v_mfma_f32_16x16x32_bf16 v[152:155], v[12:15], v[40:43], v[152:155]\n"
  "v_mfma_f32_16x16x32_bf16 v[200:203], v[12:15], v[48:51], v[200:203]\n"
  "v_mfma_f32_16x16x32_bf16 v[248:251], v[12:15], v[56:59], v[248:251]\n"
  "s_waitcnt lgkmcnt(1)\n"
  "buffer_store_dwordx4 v[112:115], v37, s[44:47], s56 offen offset:0 sc1\n"
  "s_waitcnt lgkmcnt(0)\n"
  "buffer_store_dwordx4 v[116:119], v37, s[44:47], s56 offen offset:3072 sc1\n"
  "v_mbcnt_lo_u32_b32 v38, -1, 0\n"
  "v_mbcnt_hi_u32_b32 v38, -1, v38\n"
  "v_lshlrev_b32_e32 v38, 4, v38\n"
  "v_add_u32_e32 v38, s58, v38\n"
  "s_sub_u32 s62, s58, s57\n"
  "v_add_u32_e32 v39, s62, v3\n"
  "s_waitcnt vmcnt(17)\n"
  "ds_write_b128 v38, v[64:67]\n"
  "ds_write_b128 v38, v[68:71] offset:1024\n"
  "ds_read_b128 v[64:67], v39\n"
  "ds_read_b128 v[68:71], v39 offset:512\n"
  "ds_write_b128 v38, v[88:91]\n"
  "ds_write_b128 v38, v[92:95] offset:1024\n"
  "ds_read_b128 v[88:91], v39\n"
  "ds_read_b128 v[92:95], v39 offset:512\n"
  "v_mfma_f32_16x16x32_bf16 v[252:255], v[16:19], v[56:59], v[252:255]\n"
  "ds_read_b128 v[24:27], v3 offset:0\n"
  "v_mfma_f32_16x16x32_bf16 v[204:207], v[16:19], v[48:51], v[204:207]\n"
  "ds_read_b128 v[28:31], v0 offset:2048\n"
  "v_mfma_f32_16x16x32_bf16 v[156:159], v[16:19], v[40:43], v[156:159]\n"
  "ds_read_b128 v[12:15], v0 offset:512\n"
  "s_waitcnt lgkmcnt(1)\n"
  "v_mfma_f32_16x16x32_bf16 v[40:43], v[20:23], v[64:67], 0\n"
  "ds_read_b128 v[16:19], v3 offset:512\n"
  "v_mfma_f32_16x16x32_bf16 v[48:51], v[20:23], v[88:91], 0\n"
  "v_mfma_f32_16x16x32_bf16 v[56:59], v[20:23], v[24:27], 0\n"
  "ds_read_b128 v[20:23], v0 offset:2560\n"
  "s_waitcnt vmcnt(11)\n"
  "v_mfma_f32_16x16x32_bf16 v[60:63], v[28:31], v[24:27], 0\n"
  "ds_read_b128 v[24:27], v0 offset:4096\n"
  "ds_write_b128 v38, v[72:75]\n"
  "v_mfma_f32_16x16x32_bf16 v[52:55], v[28:31], v[88:91], 0\n"
  "ds_write_b128 v38, v[76:79] offset:1024\n"
  "ds_read_b128 v[72:75], v39\n"
  "v_mfma_f32_16x16x32_bf16 v[44:47], v[28:31], v[64:67], 0\n"
  "ds_read_b128 v[28:31], v3 offset:2048\n"
  "ds_read_b128 v[76:79], v39 offset:512\n"
  "s_waitcnt lgkmcnt(6)\n"
  "v_mfma_f32_16x16x32_bf16 v[40:43], v[12:15], v[68:71], v[40:43]\n"
  "ds_write_b128 v38, v[96:99]\n"
  "ds_write_b128 v38, v[100:103] offset:1024\n"
  "v_mfma_f32_16x16x32_bf16 v[48:51], v[12:15], v[92:95], v[48:51]\n"
  "ds_read_b128 v[96:99], v39\n"
  "ds_read_b128 v[100:103], v39 offset:512\n"
  "v_mfma_f32_16x16x32_bf16 v[56:59], v[12:15], v[16:19], v[56:59]\n"
  "ds_read_b128 v[12:15], v0 offset:6144\n"
  "v_mfma_f32_16x16x32_bf16 v[60:63], v[20:23], v[16:19], v[60:63]\n"
  "ds_read_b128 v[16:19], v0 offset:4608\n"
  "v_mfma_f32_16x16x32_bf16 v[52:55], v[20:23], v[92:95], v[52:55]\n"
  "v_mfma_f32_16x16x32_bf16 v[44:47], v[20:23], v[68:71], v[44:47]\n"
  "ds_read_b128 v[20:23], v3 offset:2560\n"
  "s_waitcnt lgkmcnt(2)\n"
  "v_mfma_f32_16x16x32_bf16 v[40:43], v[24:27], v[72:75], v[40:43]\n"
  "v_mfma_f32_16x16x32_bf16 v[48:51], v[24:27], v[96:99], v[48:51]\n"
  "v_mfma_f32_16x16x32_bf16 v[56:59], v[24:27], v[28:31], v[56:59]\n"
  "ds_read_b128 v[24:27], v0 offset:6656\n"
  "s_waitcnt vmcnt(5)\n"
  "v_mfma_f32_16x16x32_bf16 v[60:63], v[12:15], v[28:31], v[60:63]\n"
  "ds_read_b128 v[28:31], v0 offset:8192\n"
  "ds_write_b128 v38, v[80:83]\n"
  "v_mfma_f32_16x16x32_bf16 v[52:55], v[12:15], v[96:99], v[52:55]\n"
  "ds_write_b128 v38, v[84:87] offset:1024\n"
  "ds_read_b128 v[80:83], v39\n"
  "v_mfma_f32_16x16x32_bf16 v[44:47], v[12:15], v[72:75], v[44:47]\n"
  "ds_read_b128 v[12:15], v3 offset:4096\n"
  "ds_read_b128 v[84:87], v39 offset:512\n"
  "s_waitcnt lgkmcnt(6)\n"
  "v_mfma_f32_16x16x32_bf16 v[40:43], v[16:19], v[76:79], v[40:43]\n"
  "ds_write_b128 v38, v[104:107]\n"
  "ds_write_b128 v38, v[108:111] offset:1024\n"
  "v_mfma_f32_16x16x32_bf16 v[48:51], v[16:19], v[100:103], v[48:51]\n"
  "ds_read_b128 v[104:107], v39\n"
  "ds_read_b128 v[108:111], v39 offset:512\n"
  "v_mfma_f32_16x16x32_bf16 v[56:59], v[16:19], v[20:23], v[56:59]\n"
  "ds_read_b128 v[16:19], v0 offset:10240\n"
  "v_mul_f32_e32 v160, v34, v160\n"
  "v_mfma_f32_16x16x32_bf16 v[60:63], v[24:27], v[20:23], v[60:63]\n"
  "ds_read_b128 v[20:23], v0 offset:8704\n"
  "v_mul_f32_e32 v161, v34, v161\n"
  "v_mfma_f32_16x16x32_bf16 v[52:55], v[24:27], v[100:103], v[52:55]\n"
  "v_mul_f32_e32 v162, v34, v162\n"
  "v_mul_f32_e32 v163, v34, v163\n"
  "v_mfma_f32_16x16x32_bf16 v[44:47], v[24:27], v[76:79], v[44:47]\n"
  "ds_read_b128 v[24:27], v3 offset:4608\n"
  "v_cvt_pk_bf16_f32 v160, v160, v161\n"
  "s_waitcnt lgkmcnt(2)\n"
  "v_mfma_f32_16x16x32_bf16 v[40:43], v[28:31], v[80:83], v[40:43]\n"
  "v_cvt_pk_bf16_f32 v161, v162, v163\n"
  "ds_write_b64 v33, v[160:161] offset:0\n"
  "v_mfma_f32_16x16x32_bf16 v[48:51], v[28:31], v[104:107], v[48:51]\n"
  "v_mul_f32_e32 v164, v34, v164\n"
  "v_mul_f32_e32 v165, v34, v165\n"
  "v_mfma_f32_16x16x32_bf16 v[56:59], v[28:31], v[12:15], v[56:59]\n"
  "ds_read_b128 v[28:31], v0 offset:10752\n"
  "v_mul_f32_e32 v166, v34, v166\n"
  "v_mfma_f32_16x16x32_bf16 v[60:63], v[16:19], v[12:15], v[60:63]\n"
  "v_mul_f32_e32 v167, v34, v167\n"
  "v_cvt_pk_bf16_f32 v164, v164, v165\n"
  "v_mfma_f32_16x16x32_bf16 v[52:55], v[16:19], v[104:107], v[52:55]\n"
  "v_cvt_pk_bf16_f32 v165, v166, v167\n"
  "ds_write_b64 v33, v[164:165] offset:32\n"
  "v_mfma_f32_16x16x32_bf16 v[44:47], v[16:19], v[80:83], v[44:47]\n"
  "v_mul_f32_e32 v168, v34, v168\n"
  "v_mul_f32_e32 v169, v34, v169\n"
  "s_waitcnt lgkmcnt(1)\n"
  "v_mfma_f32_16x16x32_bf16 v[40:43], v[20:23], v[84:87], v[40:43]\n"
  "v_mul_f32_e32 v170, v34, v170\n"
  "v_mul_f32_e32 v171, v34, v171\n"
  "v_mfma_f32_16x16x32_bf16 v[48:51], v[20:23], v[108:111], v[48:51]\n"
  "v_cvt_pk_bf16_f32 v168, v168, v169\n"
  "v_cvt_pk_bf16_f32 v169, v170, v171\n"
  "v_mfma_f32_16x16x32_bf16 v[56:59], v[20:23], v[24:27], v[56:59]\n"
  "ds_write_b64 v33, v[168:169] offset:64\n"
  "v_mul_f32_e32 v172, v34, v172\n"
  "v_mfma_f32_16x16x32_bf16 v[60:63], v[28:31], v[24:27], v[60:63]\n"
  "v_mul_f32_e32 v173, v34, v173\n"
  "v_mul_f32_e32 v174, v34, v174\n"
  "v_mfma_f32_16x16x32_bf16 v[52:55], v[28:31], v[108:111], v[52:55]\n"
  "v_mul_f32_e32 v175, v34, v175\n"
  "v_cvt_pk_bf16_f32 v172, v172, v173\n"
  "v_mfma_f32_16x16x32_bf16 v[44:47], v[28:31], v[84:87], v[44:47]\n"
  "v_cvt_pk_bf16_f32 v173, v174, v175\n"
  "ds_write_b64 v33, v[172:173] offset:96\n"
  "s_nop 7\n"
  "s_nop 1\n"
  "ds_read2_b64 v[160:163], v35 offset0:0 offset1:1\n"
  "ds_read2_b64 v[164:167], v35 offset0:136 offset1:137\n"
  "s_waitcnt lgkmcnt(1)\n"
  "buffer_store_dwordx4 v[160:163], v37, s[44:47], s75 offen offset:0 sc1\n"
  "s_waitcnt lgkmcnt(0)\n"
  "buffer_store_dwordx4 v[164:167], v37, s[44:47], s75 offen offset:3072 sc1\n"
  "s_mov_b32 s50, 16\n"
  "s_barrier\n"
  "s_add_u32 m0, s49, 0\n"
  "s_nop 0\n"
  "buffer_load_dwordx4 v4, s[36:39], s48 offen offset:0 nt lds\n"
  "s_add_u32 m0, s49, 3968\n"
  "s_nop 0\n"
  "buffer_load_dwordx4 v4, s[36:39], s48 offen offset:128 nt lds\n"
  "s_add_u32 m0, s49, 7936\n"
  "s_nop 0\n"
  "buffer_load_dwordx4 v4, s[36:39], s48 offen offset:256 nt lds\n"
  "s_and_b32 s62, s50, 3\n"
  "s_cmp_eq_u32 s62, 3\n"
  "s_cselect_b32 s54, s61, s60\n"
  "s_cmp_eq_u32 s62, 0\n"
  "s_cselect_b32 s55, s61, s60\n"
  "s_add_u32 s49, s49, s55\n"
  "s_add_u32 s48, s48, s60\n"
  "v_add_u32_e32 v0, s54, v0\n"
  "v_add_u32_e32 v1, s55, v1\n"
  "v_add_u32_e32 v2, s55, v2\n"
  "v_mul_f32_e32 v208, v36, v208\n"
  "v_mul_f32_e32 v209, v36, v209\n"
  "v_mul_f32_e32 v210, v36, v210\n"
  "v_mul_f32_e32 v211, v36, v211\n"
  "v_cvt_pk_bf16_f32 v208, v208, v209\n"
  "v_cvt_pk_bf16_f32 v209, v210, v211\n"
  "ds_write_b64 v33, v[208:209] offset:0\n"
  "v_mul_f32_e32 v212, v36, v212\n"
  "v_mul_f32_e32 v213, v36, v213\n"
  "v_mul_f32_e32 v214, v36, v214\n"
  "v_mul_f32_e32 v215, v36, v215\n"
  "v_cvt_pk_bf16_f32 v212, v212, v213\n"
  "v_cvt_pk_bf16_f32 v213, v214, v215\n"
  "ds_write_b64 v33, v[212:213] offset:32\n"
  "v_mul_f32_e32 v216, v36, v216\n"
  "v_mul_f32_e32 v217, v36, v217\n"
  "v_mul_f32_e32 v218, v36, v218\n"
  "v_mul_f32_e32 v219, v36, v219\n"
  "v_cvt_pk_bf16_f32 v216, v216, v217\n"
  "v_cvt_pk_bf16_f32 v217, v218, v219\n"
  "ds_write_b64 v33, v[216:217] offset:64\n"
  "v_mul_f32_e32 v220, v36, v220\n"
  "v_mul_f32_e32 v221, v36, v221\n"
  "v_mul_f32_e32 v222, v36, v222\n"
  "v_mul_f32_e32 v223, v36, v223\n"
  "v_cvt_pk_bf16_f32 v220, v220, v221\n"
  "v_cvt_pk_bf16_f32 v221, v222, v223\n"
  "ds_write_b64 v33, v[220:221] offset:96\n"
  "ds_read2_b64 v[208:211], v35 offset0:0 offset1:1\n"
  "ds_read2_b64 v[212:215], v35 offset0:136 offset1:137\n"
  "v_mul_f32_e32 v128, v32, v128\n"
  "v_mul_f32_e32 v129, v32, v129\n"
  "v_mul_f32_e32 v130, v32, v130\n"
  "v_mul_f32_e32 v131, v32, v131\n"
  "v_cvt_pk_bf16_f32 v128, v128, v129\n"
  "v_cvt_pk_bf16_f32 v129, v130, v131\n"
  "ds_write_b64 v33, v[128:129] offset:0\n"
  "v_mul_f32_e32 v132, v32, v132\n"
  "v_mul_f32_e32 v133, v32, v133\n"
  "s_waitcnt lgkmcnt(2)\n"
  "buffer_store_dwordx4 v[208:211], v37, s[44:47], s76 offen offset:0 sc1\n"
  "v_mul_f32_e32 v134, v32, v134\n"
  "v_mul_f32_e32 v135, v32, v135\n"
  "v_cvt_pk_bf16_f32 v132, v132, v133\n"
  "v_cvt_pk_bf16_f32 v133, v134, v135\n"
  "s_waitcnt lgkmcnt(1)\n"
  "buffer_store_dwordx4 v[212:215], v37, s[44:47], s76 offen offset:3072 sc1\n"
  "ds_write_b64 v33, v[132:133] offset:32\n"
  "v_mul_f32_e32 v136, v32, v136\n"
  "v_mul_f32_e32 v137, v32, v137\n"
  "v_mul_f32_e32 v138, v32, v138\n"
  "v_mul_f32_e32 v139, v32, v139\n"
  "v_cvt_pk_bf16_f32 v136, v136, v137\n"
  "v_cvt_pk_bf16_f32 v137, v138, v139\n"
  "ds_write_b64 v33, v[136:137] offset:64\n"
  "v_mul_f32_e32 v140, v32, v140\n"
  "v_mul_f32_e32 v141, v32, v141\n"
  "v_mul_f32_e32 v142, v32, v142\n"
  "v_mul_f32_e32 v143, v32, v143\n"
  "v_cvt_pk_bf16_f32 v140, v140, v141\n"
  "v_cvt_pk_bf16_f32 v141, v142, v143\n"
  "ds_write_b64 v33, v[140:141] offset:96\n"
  "ds_read2_b64 v[128:131], v35 offset0:0 offset1:1\n"
  "ds_read2_b64 v[132:135], v35 offset0:136 offset1:137\n"
  "v_mul_f32_e32 v176, v34, v176\n"
  "v_mul_f32_e32 v177, v34, v177\n"
  "v_mul_f32_e32 v178, v34, v178\n"
  "v_mul_f32_e32 v179, v34, v179\n"
  "v_cvt_pk_bf16_f32 v176, v176, v177\n"
  "v_cvt_pk_bf16_f32 v177, v178, v179\n"
  "ds_write_b64 v33, v[176:177] offset:0\n"
  "v_mul_f32_e32 v180, v34, v180\n"
  "v_mul_f32_e32 v181, v34, v181\n"
  "s_waitcnt lgkmcnt(2)\n"
  "buffer_store_dwordx4 v[128:131], v37, s[44:47], s56 offen offset:128 sc1\n"
  "v_mul_f32_e32 v182, v34, v182\n"
  "v_mul_f32_e32 v183, v34, v183\n"
  "v_cvt_pk_bf16_f32 v180, v180, v181\n"
  "v_cvt_pk_bf16_f32 v181, v182, v183\n"
  "s_waitcnt lgkmcnt(1)\n"
  "buffer_store_dwordx4 v[132:135], v37, s[44:47], s56 offen offset:3200 sc1\n"
  "ds_write_b64 v33, v[180:181] offset:32\n"
  "v_mul_f32_e32 v184, v34, v184\n"
  "v_mul_f32_e32 v185, v34, v185\n"
  "v_mul_f32_e32 v186, v34, v186\n"
  "v_mul_f32_e32 v187, v34, v187\n"
  "v_cvt_pk_bf16_f32 v184, v184, v185\n"
  "v_cvt_pk_bf16_f32 v185, v186, v187\n"
  "ds_write_b64 v33, v[184:185] offset:64\n"
  "v_mul_f32_e32 v188, v34, v188\n"
  "v_mul_f32_e32 v189, v34, v189\n"
  "v_mul_f32_e32 v190, v34, v190\n"
  "v_mul_f32_e32 v191, v34, v191\n"
  "v_cvt_pk_bf16_f32 v188, v188, v189\n"
  "v_cvt_pk_bf16_f32 v189, v190, v191\n"
  "ds_write_b64 v33, v[188:189] offset:96\n"
  "ds_read2_b64 v[176:179], v35 offset0:0 offset1:1\n"
  "ds_read2_b64 v[180:183], v35 offset0:136 offset1:137\n"
  "v_mul_f32_e32 v224, v36, v224\n"
  "v_mul_f32_e32 v225, v36, v225\n"
  "v_mul_f32_e32 v226, v36, v226\n"
  "v_mul_f32_e32 v227, v36, v227\n"
  "v_cvt_pk_bf16_f32 v224, v224, v225\n"
  "v_cvt_pk_bf16_f32 v225, v226, v227\n"
  "ds_write_b64 v33, v[224:225] offset:0\n"
  "v_mul_f32_e32 v228, v36, v228\n"
  "v_mul_f32_e32 v229, v36, v229\n"
  "s_waitcnt lgkmcnt(2)\n"
  "buffer_store_dwordx4 v[176:179], v37, s[44:47], s75 offen offset:128 sc1\n"
  "v_mul_f32_e32 v230, v36, v230\n"
  "v_mul_f32_e32 v231, v36, v231\n"
  "v_cvt_pk_bf16_f32 v228, v228, v229\n"
  "v_cvt_pk_bf16_f32 v229, v230, v231\n"
  "s_waitcnt lgkmcnt(1)\n"
  "buffer_store_dwordx4 v[180:183], v37, s[44:47], s75 offen offset:3200 sc1\n"
  "ds_write_b64 v33, v[228:229] offset:32\n"
  "v_mul_f32_e32 v232, v36, v232\n"
  "v_mul_f32_e32 v233, v36, v233\n"
  "v_mul_f32_e32 v234, v36, v234\n"
  "v_mul_f32_e32 v235, v36, v235\n"
  "v_cvt_pk_bf16_f32 v232, v232, v233\n"
  "v_cvt_pk_bf16_f32 v233, v234, v235\n"
  "ds_write_b64 v33, v[232:233] offset:64\n"
  "v_mul_f32_e32 v236, v36, v236\n"
  "v_mul_f32_e32 v237, v36, v237\n"
  "v_mul_f32_e32 v238, v36, v238\n"
  "v_mul_f32_e32 v239, v36, v239\n"
  "v_cvt_pk_bf16_f32 v236, v236, v237\n"
  "v_cvt_pk_bf16_f32 v237, v238, v239\n"
  "ds_write_b64 v33, v[236:237] offset:96\n"
  "ds_read2_b64 v[224:227], v35 offset0:0 offset1:1\n"
  "ds_read2_b64 v[228:231], v35 offset0:136 offset1:137\n"
  "v_max3_f32 v12, v40, v41, v42\n"
  "v_max3_f32 v13, v48, v49, v50\n"
  "v_max3_f32 v14, v56, v57, v58\n"
  "v_max3_f32 v12, v12, v43, v44\n"
  "v_max3_f32 v13, v13, v51, v52\n"
  "v_max3_f32 v14, v14, v59, v60\n"
  "v_max3_f32 v12, v12, v45, v46\n"
  "v_max3_f32 v13, v13, v53, v54\n"
  "v_max3_f32 v14, v14, v61, v62\n"
  "v_max_f32_e32 v12, v12, v47\n"
  "v_max_f32_e32 v13, v13, v55\n"
  "v_max_f32_e32 v14, v14, v63\n"
  "v_mov_b32_e32 v15, v12\n"
  "v_mov_b32_e32 v16, v13\n"
  "v_mov_b32_e32 v17, v14\n"
  "s_nop 1\n"
  "v_permlane32_swap_b32_e32 v12, v15\n"
  "v_permlane32_swap_b32_e32 v13, v16\n"
  "v_permlane32_swap_b32_e32 v14, v17\n"
  "s_nop 1\n"
  "v_max_f32_e32 v12, v12, v15\n"
  "v_max_f32_e32 v13, v13, v16\n"
  "v_max_f32_e32 v14, v14, v17\n"
  "v_mov_b32_e32 v15, v12\n"
  "v_mov_b32_e32 v16, v13\n"
  "v_mov_b32_e32 v17, v14\n"
  "s_nop 1\n"
  "v_permlane16_swap_b32_e32 v12, v15\n"
  "v_permlane16_swap_b32_e32 v13, v16\n"
  "v_permlane16_swap_b32_e32 v14, v17\n"
  "s_nop 1\n"
  "v_max_f32_e32 v12, v12, v15\n"
  "v_max_f32_e32 v13, v13, v16\n"
  "v_max_f32_e32 v14, v14, v17\n"
  "v_mul_f32_e64 v6, -v12, s52\n"
  "v_mul_f32_e64 v8, -v13, s52\n"
  "v_mul_f32_e64 v10, -v14, s52\n"
  "v_fma_f32 v40, v40, s52, v6\n"
  "v_fma_f32 v41, v41, s52, v6\n"
  "v_fma_f32 v42, v42, s52, v6\n"
  "v_fma_f32 v43, v43, s52, v6\n"
  "v_fma_f32 v48, v48, s52, v8\n"
  "v_fma_f32 v49, v49, s52, v8\n"
  "v_fma_f32 v50, v50, s52, v8\n"
  "v_fma_f32 v51, v51, s52, v8\n"
  "v_fma_f32 v56, v56, s52, v10\n"
  "v_fma_f32 v57, v57, s52, v10\n"
  "v_fma_f32 v58, v58, s52, v10\n"
  "v_fma_f32 v59, v59, s52, v10\n"
  "v_fma_f32 v60, v60, s52, v10\n"
  "v_fma_f32 v61, v61, s52, v10\n"
  "v_fma_f32 v62, v62, s52, v10\n"
  "v_fma_f32 v63, v63, s52, v10\n"
  "v_fma_f32 v52, v52, s52, v8\n"
  "v_fma_f32 v53, v53, s52, v8\n"
  "v_fma_f32 v54, v54, s52, v8\n"
  "v_fma_f32 v55, v55, s52, v8\n"
  "v_fma_f32 v44, v44, s52, v6\n"
  "v_fma_f32 v45, v45, s52, v6\n"
  "v_fma_f32 v46, v46, s52, v6\n"
  "v_fma_f32 v47, v47, s52, v6\n"
  "v_exp_f32_e32 v40, v40\n"
  "v_exp_f32_e32 v41, v41\n"
  "v_exp_f32_e32 v42, v42\n"
  "v_exp_f32_e32 v43, v43\n"
  "v_exp_f32_e32 v44, v44\n"
  "v_exp_f32_e32 v45, v45\n"
  "v_exp_f32_e32 v46, v46\n"
  "v_exp_f32_e32 v47, v47\n"
  "v_exp_f32_e32 v48, v48\n"
  "v_exp_f32_e32 v49, v49\n"
  "v_exp_f32_e32 v50, v50\n"
  "v_exp_f32_e32 v51, v51\n"
  "v_exp_f32_e32 v52, v52\n"
  "v_exp_f32_e32 v53, v53\n"
  "v_exp_f32_e32 v54, v54\n"
  "v_exp_f32_e32 v55, v55\n"
  "v_exp_f32_e32 v56, v56\n"
  "v_exp_f32_e32 v57, v57\n"
  "v_exp_f32_e32 v58, v58\n"
  "v_exp_f32_e32 v59, v59\n"
  "v_exp_f32_e32 v60, v60\n"
  "v_exp_f32_e32 v61, v61\n"
  "v_exp_f32_e32 v62, v62\n"
  "v_exp_f32_e32 v63, v63\n"
  "v_add_f32_e32 v7, v40, v41\n"
  "v_add_f32_e32 v9, v48, v49\n"
  "v_add_f32_e32 v11, v56, v57\n"
  "v_add_f32_e32 v7, v7, v42\n"
  "v_add_f32_e32 v9, v9, v50\n"
  "v_add_f32_e32 v11, v11, v58\n"
  "v_add_f32_e32 v7, v7, v43\n"
  "v_add_f32_e32 v9, v9, v51\n"
  "v_add_f32_e32 v11, v11, v59\n"
  "v_add_f32_e32 v7, v7, v44\n"
  "v_add_f32_e32 v9, v9, v52\n"
  "v_add_f32_e32 v11, v11, v60\n"
  "v_add_f32_e32 v7, v7, v45\n"
  "v_add_f32_e32 v9, v9, v53\n"
  "v_add_f32_e32 v11, v11, v61\n"
  "v_add_f32_e32 v7, v7, v46\n"
  "v_add_f32_e32 v9, v9, v54\n"
  "v_add_f32_e32 v11, v11, v62\n"
  "v_add_f32_e32 v7, v7, v47\n"
  "v_add_f32_e32 v9, v9, v55\n"
  "v_add_f32_e32 v11, v11, v63\n"
  "v_cvt_pk_bf16_f32 v40, v40, v41\n"
  "v_cvt_pk_bf16_f32 v41, v42, v43\n"
  "v_cvt_pk_bf16_f32 v42, v44, v45\n"
  "v_cvt_pk_bf16_f32 v43, v46, v47\n"
  "v_cvt_pk_bf16_f32 v48, v48, v49\n"
  "v_cvt_pk_bf16_f32 v49, v50, v51\n"
  "v_cvt_pk_bf16_f32 v50, v52, v53\n"
  "v_cvt_pk_bf16_f32 v51, v54, v55\n"
  "v_cvt_pk_bf16_f32 v56, v56, v57\n"
  "v_cvt_pk_bf16_f32 v57, v58, v59\n"
  "v_cvt_pk_bf16_f32 v58, v60, v61\n"
  "v_cvt_pk_bf16_f32 v59, v62, v63\n"
  "s_waitcnt lgkmcnt(1)\n"
  "buffer_store_dwordx4 v[224:227], v37, s[44:47], s76 offen offset:128 sc1\n"
  "s_waitcnt lgkmcnt(0)\n"
  "buffer_store_dwordx4 v[228:231], v37, s[44:47], s76 offen offset:3200 sc1\n"
  "ds_read_b64_tr_b16 v[12:13], v1 offset:0\n"
  "ds_read_b64_tr_b16 v[14:15], v1 offset:2048\n"
  "ds_read_b64_tr_b16 v[16:17], v2 offset:0\n"
  "ds_read_b64_tr_b16 v[18:19], v2 offset:2048\n"
  "ds_read_b64_tr_b16 v[20:21], v1 offset:512\n"
  "ds_read_b64_tr_b16 v[22:23], v1 offset:2560\n"
  "ds_read_b64_tr_b16 v[24:25], v2 offset:512\n"
  "ds_read_b64_tr_b16 v[26:27], v2 offset:2560\n"
  "s_barrier\n"
  "s_waitcnt lgkmcnt(4)\n"
  "v_mfma_f32_16x16x32_bf16 v[112:115], v[12:15], v[40:43], 0\n"
  "ds_read_b64_tr_b16 v[28:29], v1 offset:4096\n"
  "v_mul_f32_e32 v144, v32, v144\n"
  "v_mul_f32_e32 v145, v32, v145\n"
  "v_mul_f32_e32 v146, v32, v146\n"
  "v_mfma_f32_16x16x32_bf16 v[160:163], v[12:15], v[48:51], 0\n"
  "ds_read_b64_tr_b16 v[30:31], v1 offset:6144\n"
  "v_mul_f32_e32 v147, v32, v147\n"
  "v_cvt_pk_bf16_f32 v144, v144, v145\n"
  "v_cvt_pk_bf16_f32 v145, v146, v147\n"
  "v_mfma_f32_16x16x32_bf16 v[208:211], v[12:15], v[56:59], 0\n"
  "ds_read_b64_tr_b16 v[12:13], v2 offset:4096\n"
  "ds_write_b64 v33, v[144:145] offset:0\n"
  "v_mul_f32_e32 v148, v32, v148\n"
  "v_mul_f32_e32 v149, v32, v149\n"
  "v_mfma_f32_16x16x32_bf16 v[212:215], v[16:19], v[56:59], 0\n"
  "ds_read_b64_tr_b16 v[14:15], v2 offset:6144\n"
  "v_mul_f32_e32 v150, v32, v150\n"
  "v_mul_f32_e32 v151, v32, v151\n"
  "v_cvt_pk_bf16_f32 v148, v148, v149\n"
  "v_mfma_f32_16x16x32_bf16 v[164:167], v[16:19], v[48:51], 0\n"
  "v_cvt_pk_bf16_f32 v149, v150, v151\n"
  "ds_write_b64 v33, v[148:149] offset:32\n"
  "v_mul_f32_e32 v152, v32, v152\n"
  "v_mul_f32_e32 v153, v32, v153\n"
  "v_mfma_f32_16x16x32_bf16 v[116:119], v[16:19], v[40:43], 0\n"
  "ds_read_b64_tr_b16 v[16:17], v1 offset:4608\n"
  "v_mul_f32_e32 v154, v32, v154\n"
  "v_mul_f32_e32 v155, v32, v155\n"
  "v_cvt_pk_bf16_f32 v152, v152, v153\n"
  "s_waitcnt lgkmcnt(7)\n"
  "v_mfma_f32_16x16x32_bf16 v[120:123], v[20:23], v[40:43], 0\n"
  "ds_read_b64_tr_b16 v[18:19], v1 offset:6656\n"
  "v_cvt_pk_bf16_f32 v153, v154, v155\n"
  "ds_write_b64 v33, v[152:153] offset:64\n"
  "v_mul_f32_e32 v156, v32, v156\n"
  "v_mfma_f32_16x16x32_bf16 v[168:171], v[20:23], v[48:51], 0\n"
  "v_mul_f32_e32 v157, v32, v157\n"
  "v_mul_f32_e32 v158, v32, v158\n"
  "v_mul_f32_e32 v159, v32, v159\n"
  "v_cvt_pk_bf16_f32 v156, v156, v157\n"
  "v_mfma_f32_16x16x32_bf16 v[216:219], v[20:23], v[56:59], 0\n"
  "ds_read_b64_tr_b16 v[20:21], v2 offset:4608\n"
  "v_cvt_pk_bf16_f32 v157, v158, v159\n"
  "ds_write_b64 v33, v[156:157] offset:96\n"
  "ds_read2_b64 v[144:147], v35 offset0:0 offset1:1\n"
  "v_mfma_f32_16x16x32_bf16 v[220:223], v[24:27], v[56:59], 0\n"
  "ds_read_b64_tr_b16 v[22:23], v2 offset:6656\n"
  "ds_read2_b64 v[148:151], v35 offset0:136 offset1:137\n"
  "v_mul_f32_e32 v192, v34, v192\n"
  "v_mul_f32_e32 v193, v34, v193\n"
  "v_mfma_f32_16x16x32_bf16 v[172:175], v[24:27], v[48:51], 0\n"
  "v_mul_f32_e32 v194, v34, v194\n"
  "v_mul_f32_e32 v195, v34, v195\n"
  "v_cvt_pk_bf16_f32 v192, v192, v193\n"
  "v_cvt_pk_bf16_f32 v193, v194, v195\n"
  "v_mfma_f32_16x16x32_bf16 v[124:127], v[24:27], v[40:43], 0\n"
  "ds_read_b64_tr_b16 v[24:25], v1 offset:8192\n"
  "ds_write_b64 v33, v[192:193] offset:0\n"
  "v_mul_f32_e32 v196, v34, v196\n"
  "v_mul_f32_e32 v197, v34, v197\n"
  "s_waitcnt lgkmcnt(11)\n"
  "v_mfma_f32_16x16x32_bf16 v[128:131], v[28:31], v[40:43], 0\n"
  "ds_read_b64_tr_b16 v[26:27], v1 offset:10240\n"
  "s_waitcnt lgkmcnt(5)\n"
  "buffer_store_dwordx4 v[144:147], v37, s[44:47], s56 offen offset:256 sc1\n"
  "v_mul_f32_e32 v198, v34, v198\n"
  "v_mul_f32_e32 v199, v34, v199\n"
  "v_mfma_f32_16x16x32_bf16 v[176:179], v[28:31], v[48:51], 0\n"
  "v_cvt_pk_bf16_f32 v196, v196, v197\n"
  "v_cvt_pk_bf16_f32 v197, v198, v199\n"
  "s_waitcnt lgkmcnt(3)\n"
  "buffer_store_dwordx4 v[148:151], v37, s[44:47], s56 offen offset:3328 sc1\n"
  "ds_write_b64 v33, v[196:197] offset:32\n"
  "v_mfma_f32_16x16x32_bf16 v[224:227], v[28:31], v[56:59], 0\n"
  "ds_read_b64_tr_b16 v[28:29], v2 offset:8192\n"
  "v_mul_f32_e32 v200, v34, v200\n"
  "v_mul_f32_e32 v201, v34, v201\n"
  "v_mul_f32_e32 v202, v34, v202\n"
  "v_mfma_f32_16x16x32_bf16 v[228:231], v[12:15], v[56:59], 0\n"
  "ds_read_b64_tr_b16 v[30:31], v2 offset:10240\n"
  "v_mul_f32_e32 v203, v34, v203\n"
  "v_cvt_pk_bf16_f32 v200, v200, v201\n"
  "v_cvt_pk_bf16_f32 v201, v202, v203\n"
  "v_mfma_f32_16x16x32_bf16 v[180:183], v[12:15], v[48:51], 0\n"
  "ds_write_b64 v33, v[200:201] offset:64\n"
  "v_mul_f32_e32 v204, v34, v204\n"
  "v_mul_f32_e32 v205, v34, v205\n"
  "v_mul_f32_e32 v206, v34, v206\n"
  "v_mfma_f32_16x16x32_bf16 v[132:135], v[12:15], v[40:43], 0\n"
  "ds_read_b64_tr_b16 v[12:13], v1 offset:8704\n"
  "v_mul_f32_e32 v207, v34, v207\n"
  "v_cvt_pk_bf16_f32 v204, v204, v205\n"
  "v_cvt_pk_bf16_f32 v205, v206, v207\n"
  "v_mfma_f32_16x16x32_bf16 v[136:139], v[16:19], v[40:43], 0\n"
  "ds_read_b64_tr_b16 v[14:15], v1 offset:10752\n"
  "ds_write_b64 v33, v[204:205] offset:96\n"
  "ds_read2_b64 v[192:195], v35 offset0:0 offset1:1\n"
  "ds_read2_b64 v[196:199], v35 offset0:136 offset1:137\n"
  "v_mfma_f32_16x16x32_bf16 v[184:187], v[16:19], v[48:51], 0\n"
  "v_mul_f32_e32 v240, v36, v240\n"
  "v_mul_f32_e32 v241, v36, v241\n"
  "v_mul_f32_e32 v242, v36, v242\n"
  "v_mul_f32_e32 v243, v36, v243\n"
  "v_mfma_f32_16x16x32_bf16 v[232:235], v[16:19], v[56:59], 0\n"
  "ds_read_b64_tr_b16 v[16:17], v2 offset:8704\n"
  "v_cvt_pk_bf16_f32 v240, v240, v241\n"
  "v_cvt_pk_bf16_f32 v241, v242, v243\n"
  "ds_write_b64 v33, v[240:241] offset:0\n"
  "v_mfma_f32_16x16x32_bf16 v[236:239], v[20:23], v[56:59], 0\n"
  "ds_read_b64_tr_b16 v[18:19], v2 offset:10752\n"
  "v_mul_f32_e32 v244, v36, v244\n"
  "v_mul_f32_e32 v245, v36, v245\n"
  "s_waitcnt lgkmcnt(4)\n"
  "buffer_store_dwordx4 v[192:195], v37, s[44:47], s75 offen offset:256 sc1\n"
  "v_mfma_f32_16x16x32_bf16 v[188:191], v[20:23], v[48:51], 0\n"
  "v_mul_f32_e32 v246, v36, v246\n"
  "v_mul_f32_e32 v247, v36, v247\n"
  "v_cvt_pk_bf16_f32 v244, v244, v245\n"
  "v_cvt_pk_bf16_f32 v245, v246, v247\n"
  "v_mfma_f32_16x16x32_bf16 v[140:143], v[20:23], v[40:43], 0\n"
  "ds_read_b128 v[20:23], v0 offset:0\n"
  "s_waitcnt lgkmcnt(4)\n"
  "buffer_store_dwordx4 v[196:199], v37, s[44:47], s75 offen offset:3328 sc1\n"
  "ds_write_b64 v33, v[244:245] offset:32\n"
  "v_mul_f32_e32 v248, v36, v248\n"
  "v_mfma_f32_16x16x32_bf16 v[144:147], v[24:27], v[40:43], 0\n"
  "v_mul_f32_e32 v249, v36, v249\n"
  "v_mul_f32_e32 v250, v36, v250\n"
  "v_mul_f32_e32 v251, v36, v251\n"
  "v_cvt_pk_bf16_f32 v248, v248, v249\n"
  "v_mfma_f32_16x16x32_bf16 v[192:195], v[24:27], v[48:51], 0\n"
  "v_cvt_pk_bf16_f32 v249, v250, v251\n"
  "ds_write_b64 v33, v[248:249] offset:64\n"
  "v_mul_f32_e32 v252, v36, v252\n"
  "v_mul_f32_e32 v253, v36, v253\n"
  "v_mul_f32_e32 v254, v36, v254\n"
  "v_mul_f32_e32 v255, v36, v255\n"
  "v_cvt_pk_bf16_f32 v252, v252, v253\n"
  "v_cvt_pk_bf16_f32 v253, v254, v255\n"
  "ds_write_b64 v33, v[252:253] offset:96\n"
  "ds_read2_b64 v[240:243], v35 offset0:0 offset1:1\n"
  "ds_read2_b64 v[244:247], v35 offset0:136 offset1:137\n"
  "s_waitcnt lgkmcnt(1)\n"
  "buffer_store_dwordx4 v[240:243], v37, s[44:47], s76 offen offset:256 sc1\n"
  "s_nop 1\n"
  "v_mfma_f32_16x16x32_bf16 v[240:243], v[24:27], v[56:59], 0\n"
  "ds_read_b128 v[24:27], v3 offset:0\n"
  "s_waitcnt lgkmcnt(1)\n"
  "buffer_store_dwordx4 v[244:247], v37, s[44:47], s76 offen offset:3328 sc1\n"
  "s_nop 1\n"
  "v_mfma_f32_16x16x32_bf16 v[244:247], v[28:31], v[56:59], 0\n"
  "v_mfma_f32_16x16x32_bf16 v[196:199], v[28:31], v[48:51], 0\n"
  "v_mfma_f32_16x16x32_bf16 v[148:151], v[28:31], v[40:43], 0\n"
  "ds_read_b128 v[28:31], v0 offset:2048\n"
  "v_mfma_f32_16x16x32_bf16 v[152:155], v[12:15], v[40:43], 0\n"
  "v_mfma_f32_16x16x32_bf16 v[200:203], v[12:15], v[48:51], 0\n"
  "v_mfma_f32_16x16x32_bf16 v[248:251], v[12:15], v[56:59], 0\n"
  "ds_read_b128 v[12:15], v0 offset:512\n"
  "v_mfma_f32_16x16x32_bf16 v[252:255], v[16:19], v[56:59], 0\n"
  "v_mfma_f32_16x16x32_bf16 v[204:207], v[16:19], v[48:51], 0\n"
  "v_mfma_f32_16x16x32_bf16 v[156:159], v[16:19], v[40:43], 0\n"
  "ds_read_b128 v[16:19], v3 offset:512\n"
  "v_mfma_f32_16x16x32_bf16 v[40:43], v[20:23], v[64:67], 0\n"
  "v_mfma_f32_16x16x32_bf16 v[48:51], v[20:23], v[88:91], 0\n"
  "s_waitcnt lgkmcnt(1)\n"
  "v_mfma_f32_16x16x32_bf16 v[56:59], v[20:23], v[24:27], 0\n"
  "ds_read_b128 v[20:23], v0 offset:2560\n"
  "v_mfma_f32_16x16x32_bf16 v[60:63], v[28:31], v[24:27], 0\n"
  "ds_read_b128 v[24:27], v0 offset:4096\n"
  "v_mfma_f32_16x16x32_bf16 v[52:55], v[28:31], v[88:91], 0\n"
  "v_mfma_f32_16x16x32_bf16 v[44:47], v[28:31], v[64:67], 0\n"
  "ds_read_b128 v[28:31], v3 offset:2048\n"
  "v_mfma_f32_16x16x32_bf16 v[40:43], v[12:15], v[68:71], v[40:43]\n"
  "v_mfma_f32_16x16x32_bf16 v[48:51], v[12:15], v[92:95], v[48:51]\n"
  "s_waitcnt lgkmcnt(1)\n"
  "v_mfma_f32_16x16x32_bf16 v[56:59], v[12:15], v[16:19], v[56:59]\n"
  "ds_read_b128 v[12:15], v0 offset:6144\n"
  "v_mfma_f32_16x16x32_bf16 v[60:63], v[20:23], v[16:19], v[60:63]\n"
  "ds_read_b128 v[16:19], v0 offset:4608\n"
  "v_mfma_f32_16x16x32_bf16 v[52:55], v[20:23], v[92:95], v[52:55]\n"
  "v_mfma_f32_16x16x32_bf16 v[44:47], v[20:23], v[68:71], v[44:47]\n"
  "ds_read_b128 v[20:23], v3 offset:2560\n"
  "v_mfma_f32_16x16x32_bf16 v[40:43], v[24:27], v[72:75], v[40:43]\n"
  "v_mfma_f32_16x16x32_bf16 v[48:51], v[24:27], v[96:99], v[48:51]\n"
  "s_waitcnt lgkmcnt(1)\n"
  "v_mfma_f32_16x16x32_bf16 v[56:59], v[24:27], v[28:31], v[56:59]\n"
  "ds_read_b128 v[24:27], v0 offset:6656\n"
  "v_mfma_f32_16x16x32_bf16 v[60:63], v[12:15], v[28:31], v[60:63]\n"
  "ds_read_b128 v[28:31], v0 offset:8192\n"
  "v_mfma_f32_16x16x32_bf16 v[52:55], v[12:15], v[96:99], v[52:55]\n"
  "v_mfma_f32_16x16x32_bf16 v[44:47], v[12:15], v[72:75], v[44:47]\n"
  "ds_read_b128 v[12:15], v3 offset:4096\n"
  "v_mfma_f32_16x16x32_bf16 v[40:43], v[16:19], v[76:79], v[40:43]\n"
  "v_mfma_f32_16x16x32_bf16 v[48:51], v[16:19], v[100:103], v[48:51]\n"
  "s_waitcnt lgkmcnt(1)\n"
  "v_mfma_f32_16x16x32_bf16 v[56:59], v[16:19], v[20:23], v[56:59]\n"
  "ds_read_b128 v[16:19], v0 offset:10240\n"
  "v_mfma_f32_16x16x32_bf16 v[60:63], v[24:27], v[20:23], v[60:63]\n"
  "ds_read_b128 v[20:23], v0 offset:8704\n"
  "v_mfma_f32_16x16x32_bf16 v[52:55], v[24:27], v[100:103], v[52:55]\n"
  "v_mfma_f32_16x16x32_bf16 v[44:47], v[24:27], v[76:79], v[44:47]\n"
  "ds_read_b128 v[24:27], v3 offset:4608\n"
  "v_mfma_f32_16x16x32_bf16 v[40:43], v[28:31], v[80:83], v[40:43]\n"
  "v_mfma_f32_16x16x32_bf16 v[48:51], v[28:31], v[104:107], v[48:51]\n"
  "s_waitcnt lgkmcnt(1)\n"
  "v_mfma_f32_16x16x32_bf16 v[56:59], v[28:31], v[12:15], v[56:59]\n"
  "ds_read_b128 v[28:31], v0 offset:10752\n"
  "v_mfma_f32_16x16x32_bf16 v[60:63], v[16:19], v[12:15], v[60:63]\n"
  "v_mfma_f32_16x16x32_bf16 v[52:55], v[16:19], v[104:107], v[52:55]\n"
  "v_mfma_f32_16x16x32_bf16 v[44:47], v[16:19], v[80:83], v[44:47]\n"
  "v_mfma_f32_16x16x32_bf16 v[40:43], v[20:23], v[84:87], v[40:43]\n"
  "v_mfma_f32_16x16x32_bf16 v[48:51], v[20:23], v[108:111], v[48:51]\n"
  "s_waitcnt lgkmcnt(0)\n"
  "v_mfma_f32_16x16x32_bf16 v[56:59], v[20:23], v[24:27], v[56:59]\n"
  "v_mfma_f32_16x16x32_bf16 v[60:63], v[28:31], v[24:27], v[60:63]\n"
  "v_mfma_f32_16x16x32_bf16 v[52:55], v[28:31], v[108:111], v[52:55]\n"
  "v_mfma_f32_16x16x32_bf16 v[44:47], v[28:31], v[84:87], v[44:47]\n"
  "s_add_u32 s44, s44, 141312\n"
  "s_addc_u32 s45, s45, 0\n"
  "s_mov_b32 s50, 17\n"
  "s_branch .LH_loopa\n"
  ".LH_speciala:\n"
  "s_waitcnt vmcnt(20)\n"
  "s_branch .LH_mida\n"
  ".LH_fina:\n"
  "s_cmp_lt_u32 s59, 4\n"
  "s_cbranch_scc0 .LH_fin2a\n"
  "s_barrier\n"
  ".LH_fin2a:\n"
  "s_waitcnt lgkmcnt(4)\n"
  "v_mfma_f32_16x16x32_bf16 v[112:115], v[12:15], v[40:43], v[112:115]\n"
  "ds_read_b64_tr_b16 v[28:29], v1 offset:4096\n"
  "v_mfma_f32_16x16x32_bf16 v[160:163], v[12:15], v[48:51], v[160:163]\n"
  "ds_read_b64_tr_b16 v[30:31], v1 offset:6144\n"
  "v_mfma_f32_16x16x32_bf16 v[208:211], v[12:15], v[56:59], v[208:211]\n"
  "ds_read_b64_tr_b16 v[12:13], v2 offset:4096\n"
  "v_mfma_f32_16x16x32_bf16 v[212:215], v[16:19], v[56:59], v[212:215]\n"
  "ds_read_b64_tr_b16 v[14:15], v2 offset:6144\n"
  "v_mfma_f32_16x16x32_bf16 v[164:167], v[16:19], v[48:51], v[164:167]\n"
  "v_mfma_f32_16x16x32_bf16 v[116:119], v[16:19], v[40:43], v[116:119]\n"
  "ds_read_b64_tr_b16 v[16:17], v1 offset:4608\n"
  "v_mul_f32_e32 v112, v32, v112\n"
  "v_mul_f32_e32 v113, v32, v113\n"
  "s_waitcnt lgkmcnt(5)\n"
  "v_mfma_f32_16x16x32_bf16 v[120:123], v[20:23], v[40:43], v[120:123]\n"
  "ds_read_b64_tr_b16 v[18:19], v1 offset:6656\n"
  "v_mul_f32_e32 v114, v32, v114\n"
  "v_mul_f32_e32 v115, v32, v115\n"
  "v_mfma_f32_16x16x32_bf16 v[168:171], v[20:23], v[48:51], v[168:171]\n"
  "v_cvt_pk_bf16_f32 v112, v112, v113\n"
  "v_cvt_pk_bf16_f32 v113, v114, v115\n"
  "ds_write_b64 v33, v[112:113] offset:0\n"
  "v_mfma_f32_16x16x32_bf16 v[216:219], v[20:23], v[56:59], v[216:219]\n"
  "ds_read_b64_tr_b16 v[20:21], v2 offset:4608\n"
  "v_mul_f32_e32 v116, v32, v116\n"
  "v_mul_f32_e32 v117, v32, v117\n"
  "v_mfma_f32_16x16x32_bf16 v[220:223], v[24:27], v[56:59], v[220:223]\n"
  "ds_read_b64_tr_b16 v[22:23], v2 offset:6656\n"
  "v_mul_f32_e32 v118, v32, v118\n"
  "v_mul_f32_e32 v119, v32, v119\n"
  "v_mfma_f32_16x16x32_bf16 v[172:175], v[24:27], v[48:51], v[172:175]\n"
  "v_cvt_pk_bf16_f32 v116, v116, v117\n"
  "v_cvt_pk_bf16_f32 v117, v118, v119\n"
  "ds_write_b64 v33, v[116:117] offset:32\n"
  "v_mfma_f32_16x16x32_bf16 v[124:127], v[24:27], v[40:43], v[124:127]\n"
  "ds_read_b64_tr_b16 v[24:25], v1 offset:8192\n"
  "v_mul_f32_e32 v120, v32, v120\n"
  "v_mul_f32_e32 v121, v32, v121\n"
  "s_waitcnt lgkmcnt(7)\n"
  "v_mfma_f32_16x16x32_bf16 v[128:131], v[28:31], v[40:43], v[128:131]\n"
  "ds_read_b64_tr_b16 v[26:27], v1 offset:10240\n"
  "v_mul_f32_e32 v122, v32, v122\n"
  "v_mul_f32_e32 v123, v32, v123\n"
  "v_mfma_f32_16x16x32_bf16 v[176:179], v[28:31], v[48:51], v[176:179]\n"
  "v_cvt_pk_bf16_f32 v120, v120, v121\n"
  "v_cvt_pk_bf16_f32 v121, v122, v123\n"
  "ds_write_b64 v33, v[120:121] offset:64\n"
  "v_mfma_f32_16x16x32_bf16 v[224:227], v[28:31], v[56:59], v[224:227]\n"
  "ds_read_b64_tr_b16 v[28:29], v2 offset:8192\n"
  "v_mul_f32_e32 v124, v32, v124\n"
  "v_mul_f32_e32 v125, v32, v125\n"
  "v_mfma_f32_16x16x32_bf16 v[228:231], v[12:15], v[56:59], v[228:231]\n"
  "ds_read_b64_tr_b16 v[30:31], v2 offset:10240\n"
  "v_mul_f32_e32 v126, v32, v126\n"
  "v_mul_f32_e32 v127, v32, v127\n"
  "v_mfma_f32_16x16x32_bf16 v[180:183], v[12:15], v[48:51], v[180:183]\n"
  "v_cvt_pk_bf16_f32 v124, v124, v125\n"
  "v_cvt_pk_bf16_f32 v125, v126, v127\n"
  "ds_write_b64 v33, v[124:125] offset:96\n"
  "v_mfma_f32_16x16x32_bf16 v[132:135], v[12:15], v[40:43], v[132:135]\n"
  "ds_read_b64_tr_b16 v[12:13], v1 offset:8704\n"
  "ds_read2_b64 v[112:115], v35 offset0:0 offset1:1\n"
  "ds_read2_b64 v[116:119], v35 offset0:136 offset1:137\n"
  "s_waitcnt lgkmcnt(10)\n"
  "v_mfma_f32_16x16x32_bf16 v[136:139], v[16:19], v[40:43], v[136:139]\n"
  "ds_read_b64_tr_b16 v[14:15], v1 offset:10752\n"
  "v_mul_f32_e32 v160, v34, v160\n"
  "v_mul_f32_e32 v161, v34, v161\n"
  "v_mfma_f32_16x16x32_bf16 v[184:187], v[16:19], v[48:51], v[184:187]\n"
  "v_mul_f32_e32 v162, v34, v162\n"
  "v_mul_f32_e32 v163, v34, v163\n"
  "v_cvt_pk_bf16_f32 v160, v160, v161\n"
  "v_mfma_f32_16x16x32_bf16 v[232:235], v[16:19], v[56:59], v[232:235]\n"
  "ds_read_b64_tr_b16 v[16:17], v2 offset:8704\n"
  "v_cvt_pk_bf16_f32 v161, v162, v163\n"
  "ds_write_b64 v33, v[160:161] offset:0\n"
  "v_mfma_f32_16x16x32_bf16 v[236:239], v[20:23], v[56:59], v[236:239]\n"
  "ds_read_b64_tr_b16 v[18:19], v2 offset:10752\n"
  "v_mul_f32_e32 v164, v34, v164\n"
  "v_mul_f32_e32 v165, v34, v165\n"
  "v_mfma_f32_16x16x32_bf16 v[188:191], v[20:23], v[48:51], v[188:191]\n"
  "s_waitcnt lgkmcnt(5)\n"
  "buffer_store_dwordx4 v[112:115], v37, s[44:47], s56 offen offset:0 sc1\n"
  "v_mul_f32_e32 v166, v34, v166\n"
  "v_mul_f32_e32 v167, v34, v167\n"
  "v_mfma_f32_16x16x32_bf16 v[140:143], v[20:23], v[40:43], v[140:143]\n"
  "v_cvt_pk_bf16_f32 v164, v164, v165\n"
  "v_cvt_pk_bf16_f32 v165, v166, v167\n"
  "s_waitcnt lgkmcnt(4)\n"
  "buffer_store_dwordx4 v[116:119], v37, s[44:47], s56 offen offset:3072 sc1\n"
  "v_mfma_f32_16x16x32_bf16 v[144:147], v[24:27], v[40:43], v[144:147]\n"
  "ds_write_b64 v33, v[164:165] offset:32\n"
  "v_mul_f32_e32 v168, v34, v168\n"
  "v_mul_f32_e32 v169, v34, v169\n"
  "v_mfma_f32_16x16x32_bf16 v[192:195], v[24:27], v[48:51], v[192:195]\n"
  "v_mul_f32_e32 v170, v34, v170\n"
  "v_mul_f32_e32 v171, v34, v171\n"
  "v_cvt_pk_bf16_f32 v168, v168, v169\n"
  "v_mfma_f32_16x16x32_bf16 v[240:243], v[24:27], v[56:59], v[240:243]\n"
  "v_cvt_pk_bf16_f32 v169, v170, v171\n"
  "ds_write_b64 v33, v[168:169] offset:64\n"
  "v_mul_f32_e32 v172, v34, v172\n"
  "v_mfma_f32_16x16x32_bf16 v[244:247], v[28:31], v[56:59], v[244:247]\n"
  "v_mul_f32_e32 v173, v34, v173\n"
  "v_mul_f32_e32 v174, v34, v174\n"
  "v_mul_f32_e32 v175, v34, v175\n"
  "v_mfma_f32_16x16x32_bf16 v[196:199], v[28:31], v[48:51], v[196:199]\n"
  "v_cvt_pk_bf16_f32 v172, v172, v173\n"
  "v_cvt_pk_bf16_f32 v173, v174, v175\n"
  "ds_write_b64 v33, v[172:173] offset:96\n"
  "v_mfma_f32_16x16x32_bf16 v[148:151], v[28:31], v[40:43], v[148:151]\n"
  "ds_read2_b64 v[160:163], v35 offset0:0 offset1:1\n"
  "ds_read2_b64 v[164:167], v35 offset0:136 offset1:137\n"
  "v_mul_f32_e32 v208, v36, v208\n"
  "s_waitcnt lgkmcnt(5)\n"
  "v_mfma_f32_16x16x32_bf16 v[152:155], v[12:15], v[40:43], v[152:155]\n"
  "v_mul_f32_e32 v209, v36, v209\n"
  "v_mul_f32_e32 v210, v36, v210\n"
  "v_mul_f32_e32 v211, v36, v211\n"
  "v_mfma_f32_16x16x32_bf16 v[200:203], v[12:15], v[48:51], v[200:203]\n"
  "v_cvt_pk_bf16_f32 v208, v208, v209\n"
  "v_cvt_pk_bf16_f32 v209, v210, v211\n"
  "ds_write_b64 v33, v[208:209] offset:0\n"
  "v_mfma_f32_16x16x32_bf16 v[248:251], v[12:15], v[56:59], v[248:251]\n"
  "v_mul_f32_e32 v212, v36, v212\n"
  "v_mul_f32_e32 v213, v36, v213\n"
  "s_waitcnt lgkmcnt(2)\n"
  "buffer_store_dwordx4 v[160:163], v37, s[44:47], s75 offen offset:0 sc1\n"
  "v_mfma_f32_16x16x32_bf16 v[252:255], v[16:19], v[56:59], v[252:255]\n"
  "v_mul_f32_e32 v214, v36, v214\n"
  "v_mul_f32_e32 v215, v36, v215\n"
  "v_cvt_pk_bf16_f32 v212, v212, v213\n"
  "v_mfma_f32_16x16x32_bf16 v[204:207], v[16:19], v[48:51], v[204:207]\n"
  "v_cvt_pk_bf16_f32 v213, v214, v215\n"
  "s_waitcnt lgkmcnt(1)\n"
  "buffer_store_dwordx4 v[164:167], v37, s[44:47], s75 offen offset:3072 sc1\n"
  "ds_write_b64 v33, v[212:213] offset:32\n"
  "v_mfma_f32_16x16x32_bf16 v[156:159], v[16:19], v[40:43], v[156:159]\n"
  "v_mul_f32_e32 v216, v36, v216\n"
  "v_mul_f32_e32 v217, v36, v217\n"
  "v_mul_f32_e32 v218, v36, v218\n"
  "s_nop 7\n"
  "s_nop 1\n"
  "v_mul_f32_e32 v219, v36, v219\n"
  "v_cvt_pk_bf16_f32 v216, v216, v217\n"
  "v_cvt_pk_bf16_f32 v217, v218, v219\n"
  "ds_write_b64 v33, v[216:217] offset:64\n"
  "v_mul_f32_e32 v220, v36, v220\n"
  "v_mul_f32_e32 v221, v36, v221\n"
  "v_mul_f32_e32 v222, v36, v222\n"
  "v_mul_f32_e32 v223, v36, v223\n"
  "v_cvt_pk_bf16_f32 v220, v220, v221\n"
  "v_cvt_pk_bf16_f32 v221, v222, v223\n"
  "ds_write_b64 v33, v[220:221] offset:96\n"
  "ds_read2_b64 v[208:211], v35 offset0:0 offset1:1\n"
  "ds_read2_b64 v[212:215], v35 offset0:136 offset1:137\n"
  "v_mul_f32_e32 v128, v32, v128\n"
  "v_mul_f32_e32 v129, v32, v129\n"
  "v_mul_f32_e32 v130, v32, v130\n"
  "v_mul_f32_e32 v131, v32, v131\n"
  "v_cvt_pk_bf16_f32 v128, v128, v129\n"
  "v_cvt_pk_bf16_f32 v129, v130, v131\n"
  "ds_write_b64 v33, v[128:129] offset:0\n"
  "v_mul_f32_e32 v132, v32, v132\n"
  "v_mul_f32_e32 v133, v32, v133\n"
  "s_waitcnt lgkmcnt(2)\n"
  "buffer_store_dwordx4 v[208:211], v37, s[44:47], s76 offen offset:0 sc1\n"
  "v_mul_f32_e32 v134, v32, v134\n"
  "v_mul_f32_e32 v135, v32, v135\n"
  "v_cvt_pk_bf16_f32 v132, v132, v133\n"
  "v_cvt_pk_bf16_f32 v133, v134, v135\n"
  "s_waitcnt lgkmcnt(1)\n"
  "buffer_store_dwordx4 v[212:215], v37, s[44:47], s76 offen offset:3072 sc1\n"
  "ds_write_b64 v33, v[132:133] offset:32\n"
  "v_mul_f32_e32 v136, v32, v136\n"
  "v_mul_f32_e32 v137, v32, v137\n"
  "v_mul_f32_e32 v138, v32, v138\n"
  "v_mul_f32_e32 v139, v32, v139\n"
  "v_cvt_pk_bf16_f32 v136, v136, v137\n"
  "v_cvt_pk_bf16_f32 v137, v138, v139\n"
  "ds_write_b64 v33, v[136:137] offset:64\n"
  "v_mul_f32_e32 v140, v32, v140\n"
  "v_mul_f32_e32 v141, v32, v141\n"
  "v_mul_f32_e32 v142, v32, v142\n"
  "v_mul_f32_e32 v143, v32, v143\n"
  "v_cvt_pk_bf16_f32 v140, v140, v141\n"
  "v_cvt_pk_bf16_f32 v141, v142, v143\n"
  "ds_write_b64 v33, v[140:141] offset:96\n"
  "ds_read2_b64 v[128:131], v35 offset0:0 offset1:1\n"
  "ds_read2_b64 v[132:135], v35 offset0:136 offset1:137\n"
  "v_mul_f32_e32 v176, v34, v176\n"
  "v_mul_f32_e32 v177, v34, v177\n"
  "v_mul_f32_e32 v178, v34, v178\n"
  "v_mul_f32_e32 v179, v34, v179\n"
  "v_cvt_pk_bf16_f32 v176, v176, v177\n"
  "v_cvt_pk_bf16_f32 v177, v178, v179\n"
  "ds_write_b64 v33, v[176:177] offset:0\n"
  "v_mul_f32_e32 v180, v34, v180\n"
  "v_mul_f32_e32 v181, v34, v181\n"
  "s_waitcnt lgkmcnt(2)\n"
  "buffer_store_dwordx4 v[128:131], v37, s[44:47], s56 offen offset:128 sc1\n"
  "v_mul_f32_e32 v182, v34, v182\n"
  "v_mul_f32_e32 v183, v34, v183\n"
  "v_cvt_pk_bf16_f32 v180, v180, v181\n"
  "v_cvt_pk_bf16_f32 v181, v182, v183\n"
  "s_waitcnt lgkmcnt(1)\n"
  "buffer_store_dwordx4 v[132:135], v37, s[44:47], s56 offen offset:3200 sc1\n"
  "ds_write_b64 v33, v[180:181] offset:32\n"
  "v_mul_f32_e32 v184, v34, v184\n"
  "v_mul_f32_e32 v185, v34, v185\n"
  "v_mul_f32_e32 v186, v34, v186\n"
  "v_mul_f32_e32 v187, v34, v187\n"
  "v_cvt_pk_bf16_f32 v184, v184, v185\n"
  "v_cvt_pk_bf16_f32 v185, v186, v187\n"
  "ds_write_b64 v33, v[184:185] offset:64\n"
  "v_mul_f32_e32 v188, v34, v188\n"
  "v_mul_f32_e32 v189, v34, v189\n"
  "v_mul_f32_e32 v190, v34, v190\n"
  "v_mul_f32_e32 v191, v34, v191\n"
  "v_cvt_pk_bf16_f32 v188, v188, v189\n"
  "v_cvt_pk_bf16_f32 v189, v190, v191\n"
  "ds_write_b64 v33, v[188:189] offset:96\n"
  "ds_read2_b64 v[176:179], v35 offset0:0 offset1:1\n"
  "ds_read2_b64 v[180:183], v35 offset0:136 offset1:137\n"
  "v_mul_f32_e32 v224, v36, v224\n"
  "v_mul_f32_e32 v225, v36, v225\n"
  "v_mul_f32_e32 v226, v36, v226\n"
  "v_mul_f32_e32 v227, v36, v227\n"
  "v_cvt_pk_bf16_f32 v224, v224, v225\n"
  "v_cvt_pk_bf16_f32 v225, v226, v227\n"
  "ds_write_b64 v33, v[224:225] offset:0\n"
  "v_mul_f32_e32 v228, v36, v228\n"
  "v_mul_f32_e32 v229, v36, v229\n"
  "s_waitcnt lgkmcnt(2)\n"
  "buffer_store_dwordx4 v[176:179], v37, s[44:47], s75 offen offset:128 sc1\n"
  "v_mul_f32_e32 v230, v36, v230\n"
  "v_mul_f32_e32 v231, v36, v231\n"
  "v_cvt_pk_bf16_f32 v228, v228, v229\n"
  "v_cvt_pk_bf16_f32 v229, v230, v231\n"
  "s_waitcnt lgkmcnt(1)\n"
  "buffer_store_dwordx4 v[180:183], v37, s[44:47], s75 offen offset:3200 sc1\n"
  "ds_write_b64 v33, v[228:229] offset:32\n"
  "v_mul_f32_e32 v232, v36, v232\n"
  "v_mul_f32_e32 v233, v36, v233\n"
  "v_mul_f32_e32 v234, v36, v234\n"
  "v_mul_f32_e32 v235, v36, v235\n"
  "v_cvt_pk_bf16_f32 v232, v232, v233\n"
  "v_cvt_pk_bf16_f32 v233, v234, v235\n"
  "ds_write_b64 v33, v[232:233] offset:64\n"
  "v_mul_f32_e32 v236, v36, v236\n"
  "v_mul_f32_e32 v237, v36, v237\n"
  "v_mul_f32_e32 v238, v36, v238\n"
  "v_mul_f32_e32 v239, v36, v239\n"
  "v_cvt_pk_bf16_f32 v236, v236, v237\n"
  "v_cvt_pk_bf16_f32 v237, v238, v239\n"
  "ds_write_b64 v33, v[236:237] offset:96\n"
  "ds_read2_b64 v[224:227], v35 offset0:0 offset1:1\n"
  "ds_read2_b64 v[228:231], v35 offset0:136 offset1:137\n"
  "v_mul_f32_e32 v144, v32, v144\n"
  "v_mul_f32_e32 v145, v32, v145\n"
  "v_mul_f32_e32 v146, v32, v146\n"
  "v_mul_f32_e32 v147, v32, v147\n"
  "v_cvt_pk_bf16_f32 v144, v144, v145\n"
  "v_cvt_pk_bf16_f32 v145, v146, v147\n"
  "ds_write_b64 v33, v[144:145] offset:0\n"
  "v_mul_f32_e32 v148, v32, v148\n"
  "v_mul_f32_e32 v149, v32, v149\n"
  "s_waitcnt lgkmcnt(2)\n"
  "buffer_store_dwordx4 v[224:227], v37, s[44:47], s76 offen offset:128 sc1\n"
  "v_mul_f32_e32 v150, v32, v150\n"
  "v_mul_f32_e32 v151, v32, v151\n"
  "v_cvt_pk_bf16_f32 v148, v148, v149\n"
  "v_cvt_pk_bf16_f32 v149, v150, v151\n"
  "s_waitcnt lgkmcnt(1)\n"
  "buffer_store_dwordx4 v[228:231], v37, s[44:47], s76 offen offset:3200 sc1\n"
  "ds_write_b64 v33, v[148:149] offset:32\n"
  "v_mul_f32_e32 v152, v32, v152\n"
  "v_mul_f32_e32 v153, v32, v153\n"
  "v_mul_f32_e32 v154, v32, v154\n"
  "v_mul_f32_e32 v155, v32, v155\n"
  "v_cvt_pk_bf16_f32 v152, v152, v153\n"
  "v_cvt_pk_bf16_f32 v153, v154, v155\n"
  "ds_write_b64 v33, v[152:153] offset:64\n"
  "v_mul_f32_e32 v156, v32, v156\n"
  "v_mul_f32_e32 v157, v32, v157\n"
  "v_mul_f32_e32 v158, v32, v158\n"
  "v_mul_f32_e32 v159, v32, v159\n"
  "v_cvt_pk_bf16_f32 v156, v156, v157\n"
  "v_cvt_pk_bf16_f32 v157, v158, v159\n"
  "ds_write_b64 v33, v[156:157] offset:96\n"
  "ds_read2_b64 v[144:147], v35 offset0:0 offset1:1\n"
  "ds_read2_b64 v[148:151], v35 offset0:136 offset1:137\n"
  "v_mul_f32_e32 v192, v34, v192\n"
  "v_mul_f32_e32 v193, v34, v193\n"
  "v_mul_f32_e32 v194, v34, v194\n"
  "v_mul_f32_e32 v195, v34, v195\n"
  "v_cvt_pk_bf16_f32 v192, v192, v193\n"
  "v_cvt_pk_bf16_f32 v193, v194, v195\n"
  "ds_write_b64 v33, v[192:193] offset:0\n"
  "v_mul_f32_e32 v196, v34, v196\n"
  "v_mul_f32_e32 v197, v34, v197\n"
  "s_waitcnt lgkmcnt(2)\n"
  "buffer_store_dwordx4 v[144:147], v37, s[44:47], s56 offen offset:256 sc1\n"
  "v_mul_f32_e32 v198, v34, v198\n"
  "v_mul_f32_e32 v199, v34, v199\n"
  "v_cvt_pk_bf16_f32 v196, v196, v197\n"
  "v_cvt_pk_bf16_f32 v197, v198, v199\n"
  "s_waitcnt lgkmcnt(1)\n"
  "buffer_store_dwordx4 v[148:151], v37, s[44:47], s56 offen offset:3328 sc1\n"
  "ds_write_b64 v33, v[196:197] offset:32\n"
  "v_mul_f32_e32 v200, v34, v200\n"
  "v_mul_f32_e32 v201, v34, v201\n"
  "v_mul_f32_e32 v202, v34, v202\n"
  "v_mul_f32_e32 v203, v34, v203\n"
  "v_cvt_pk_bf16_f32 v200, v200, v201\n"
  "v_cvt_pk_bf16_f32 v201, v202, v203\n"
  "ds_write_b64 v33, v[200:201] offset:64\n"
  "v_mul_f32_e32 v204, v34, v204\n"
  "v_mul_f32_e32 v205, v34, v205\n"
  "v_mul_f32_e32 v206, v34, v206\n"
  "v_mul_f32_e32 v207, v34, v207\n"
  "v_cvt_pk_bf16_f32 v204, v204, v205\n"
  "v_cvt_pk_bf16_f32 v205, v206, v207\n"
  "ds_write_b64 v33, v[204:205] offset:96\n"
  "ds_read2_b64 v[192:195], v35 offset0:0 offset1:1\n"
  "ds_read2_b64 v[196:199], v35 offset0:136 offset1:137\n"
  "v_mul_f32_e32 v240, v36, v240\n"
  "v_mul_f32_e32 v241, v36, v241\n"
  "v_mul_f32_e32 v242, v36, v242\n"
  "v_mul_f32_e32 v243, v36, v243\n"
  "v_cvt_pk_bf16_f32 v240, v240, v241\n"
  "v_cvt_pk_bf16_f32 v241, v242, v243\n"
  "ds_write_b64 v33, v[240:241] offset:0\n"
  "v_mul_f32_e32 v244, v36, v244\n"
  "v_mul_f32_e32 v245, v36, v245\n"
  "s_waitcnt lgkmcnt(2)\n"
  "buffer_store_dwordx4 v[192:195], v37, s[44:47], s75 offen offset:256 sc1\n"
  "v_mul_f32_e32 v246, v36, v246\n"
  "v_mul_f32_e32 v247, v36, v247\n"
  "v_cvt_pk_bf16_f32 v244, v244, v245\n"
  "v_cvt_pk_bf16_f32 v245, v246, v247\n"
  "s_waitcnt lgkmcnt(1)\n"
  "buffer_store_dwordx4 v[196:199], v37, s[44:47], s75 offen offset:3328 sc1\n"
  "ds_write_b64 v33, v[244:245] offset:32\n"
  "v_mul_f32_e32 v248, v36, v248\n"
  "v_mul_f32_e32 v249, v36, v249\n"
  "v_mul_f32_e32 v250, v36, v250\n"
  "v_mul_f32_e32 v251, v36, v251\n"
  "v_cvt_pk_bf16_f32 v248, v248, v249\n"
  "v_cvt_pk_bf16_f32 v249, v250, v251\n"
  "ds_write_b64 v33, v[248:249] offset:64\n"
  "v_mul_f32_e32 v252, v36, v252\n"
  "v_mul_f32_e32 v253, v36, v253\n"
  "v_mul_f32_e32 v254, v36, v254\n"
  "v_mul_f32_e32 v255, v36, v255\n"
  "v_cvt_pk_bf16_f32 v252, v252, v253\n"
  "v_cvt_pk_bf16_f32 v253, v254, v255\n"
  "ds_write_b64 v33, v[252:253] offset:96\n"
  "ds_read2_b64 v[240:243], v35 offset0:0 offset1:1\n"
  "ds_read2_b64 v[244:247], v35 offset0:136 offset1:137\n"
  "s_waitcnt lgkmcnt(1)\n"
  "buffer_store_dwordx4 v[240:243], v37, s[44:47], s76 offen offset:256 sc1\n"
  "s_waitcnt lgkmcnt(0)\n"
  "buffer_store_dwordx4 v[244:247], v37, s[44:47], s76 offen offset:3328 sc1\n"
  "s_branch .LH_end\n"
  ".LH_slowa:\n"
  "v_max3_f32 v36, v40, v41, v42\n"
  "v_max3_f32 v37, v48, v49, v50\n"
  "v_max3_f32 v38, v56, v57, v58\n"
  "v_max3_f32 v36, v36, v43, v44\n"
  "v_max3_f32 v37, v37, v51, v52\n"
  "v_max3_f32 v38, v38, v59, v60\n"
  "v_max3_f32 v36, v36, v45, v46\n"
  "v_max3_f32 v37, v37, v53, v54\n"
  "v_max3_f32 v38, v38, v61, v62\n"
  "v_max_f32_e32 v36, v36, v47\n"
  "v_max_f32_e32 v37, v37, v55\n"
  "v_max_f32_e32 v38, v38, v63\n"
  "v_mov_b32_e32 v12, v36\n"
  "v_mov_b32_e32 v13, v37\n"
  "v_mov_b32_e32 v14, v38\n"
  "s_nop 1\n"
  "v_permlane32_swap_b32_e32 v36, v12\n"
  "v_permlane32_swap_b32_e32 v37, v13\n"
  "v_permlane32_swap_b32_e32 v38, v14\n"
  "s_nop 1\n"
  "v_max_f32_e32 v36, v36, v12\n"
  "v_max_f32_e32 v37, v37, v13\n"
  "v_max_f32_e32 v38, v38, v14\n"
  "v_mov_b32_e32 v12, v36\n"
  "v_mov_b32_e32 v13, v37\n"
  "v_mov_b32_e32 v14, v38\n"
  "s_nop 1\n"
  "v_permlane16_swap_b32_e32 v36, v12\n"
  "v_permlane16_swap_b32_e32 v37, v13\n"
  "v_permlane16_swap_b32_e32 v38, v14\n"
  "s_nop 1\n"
  "v_max_f32_e32 v36, v36, v12\n"
  "v_max_f32_e32 v37, v37, v13\n"
  "v_max_f32_e32 v38, v38, v14\n"
  "v_max_f32_e32 v36, 0, v36\n"
  "v_max_f32_e32 v37, 0, v37\n"
  "v_max_f32_e32 v38, 0, v38\n"
  "v_sub_f32_e32 v6, v6, v36\n"
  "v_sub_f32_e32 v8, v8, v37\n"
  "v_sub_f32_e32 v10, v10, v38\n"
  "v_exp_f32_e64 v15, -v36\n"
  "v_exp_f32_e64 v16, -v37\n"
  "v_exp_f32_e64 v17, -v38\n"
  "v_sub_f32_e32 v40, v40, v36\n"
  "v_sub_f32_e32 v41, v41, v36\n"
  "v_sub_f32_e32 v42, v42, v36\n"
  "v_sub_f32_e32 v43, v43, v36\n"
  "v_sub_f32_e32 v44, v44, v36\n"
  "v_sub_f32_e32 v45, v45, v36\n"
  "v_sub_f32_e32 v46, v46, v36\n"
  "v_sub_f32_e32 v47, v47, v36\n"
  "v_sub_f32_e32 v48, v48, v37\n"
  "v_sub_f32_e32 v49, v49, v37\n"
  "v_sub_f32_e32 v50, v50, v37\n"
  "v_sub_f32_e32 v51, v51, v37\n"
  "v_sub_f32_e32 v52, v52, v37\n"
  "v_sub_f32_e32 v53, v53, v37\n"
  "v_sub_f32_e32 v54, v54, v37\n"
  "v_sub_f32_e32 v55, v55, v37\n"
  "v_sub_f32_e32 v56, v56, v38\n"
  "v_sub_f32_e32 v57, v57, v38\n"
  "v_sub_f32_e32 v58, v58, v38\n"
  "v_sub_f32_e32 v59, v59, v38\n"
  "v_sub_f32_e32 v60, v60, v38\n"
  "v_sub_f32_e32 v61, v61, v38\n"
  "v_sub_f32_e32 v62, v62, v38\n"
  "v_sub_f32_e32 v63, v63, v38\n"
  "v_mul_f32_e32 v7, v7, v15\n"
  "v_mul_f32_e32 v112, v112, v15\n"
  "v_mul_f32_e32 v113, v113, v15\n"
  "v_mul_f32_e32 v114, v114, v15\n"
  "v_mul_f32_e32 v115, v115, v15\n"
  "v_mul_f32_e32 v116, v116, v15\n"
  "v_mul_f32_e32 v117, v117, v15\n"
  "v_mul_f32_e32 v118, v118, v15\n"
  "v_mul_f32_e32 v119, v119, v15\n"
  "v_mul_f32_e32 v120, v120, v15\n"
  "v_mul_f32_e32 v121, v121, v15\n"
  "v_mul_f32_e32 v122, v122, v15\n"
  "v_mul_f32_e32 v123, v123, v15\n"
  "v_mul_f32_e32 v124, v124, v15\n"
  "v_mul_f32_e32 v125, v125, v15\n"
  "v_mul_f32_e32 v126, v126, v15\n"
  "v_mul_f32_e32 v127, v127, v15\n"
  "v_mul_f32_e32 v128, v128, v15\n"
  "v_mul_f32_e32 v129, v129, v15\n"
  "v_mul_f32_e32 v130, v130, v15\n"
  "v_mul_f32_e32 v131, v131, v15\n"
  "v_mul_f32_e32 v132, v132, v15\n"
  "v_mul_f32_e32 v133, v133, v15\n"
  "v_mul_f32_e32 v134, v134, v15\n"
  "v_mul_f32_e32 v135, v135, v15\n"
  "v_mul_f32_e32 v136, v136, v15\n"
  "v_mul_f32_e32 v137, v137, v15\n"
  "v_mul_f32_e32 v138, v138, v15\n"
  "v_mul_f32_e32 v139, v139, v15\n"
  "v_mul_f32_e32 v140, v140, v15\n"
  "v_mul_f32_e32 v141, v141, v15\n"
  "v_mul_f32_e32 v142, v142, v15\n"
  "v_mul_f32_e32 v143, v143, v15\n"
  "v_mul_f32_e32 v144, v144, v15\n"
  "v_mul_f32_e32 v145, v145, v15\n"
  "v_mul_f32_e32 v146, v146, v15\n"
  "v_mul_f32_e32 v147, v147, v15\n"
  "v_mul_f32_e32 v148, v148, v15\n"
  "v_mul_f32_e32 v149, v149, v15\n"
  "v_mul_f32_e32 v150, v150, v15\n"
  "v_mul_f32_e32 v151, v151, v15\n"
  "v_mul_f32_e32 v152, v152, v15\n"
  "v_mul_f32_e32 v153, v153, v15\n"
  "v_mul_f32_e32 v154, v154, v15\n"
  "v_mul_f32_e32 v155, v155, v15\n"
  "v_mul_f32_e32 v156, v156, v15\n"
  "v_mul_f32_e32 v157, v157, v15\n"
  "v_mul_f32_e32 v158, v158, v15\n"
  "v_mul_f32_e32 v159, v159, v15\n"
  "v_mul_f32_e32 v9, v9, v16\n"
  "v_mul_f32_e32 v160, v160, v16\n"
  "v_mul_f32_e32 v161, v161, v16\n"
  "v_mul_f32_e32 v162, v162, v16\n"
  "v_mul_f32_e32 v163, v163, v16\n"
  "v_mul_f32_e32 v164, v164, v16\n"
  "v_mul_f32_e32 v165, v165, v16\n"
  "v_mul_f32_e32 v166, v166, v16\n"
  "v_mul_f32_e32 v167, v167, v16\n"
  "v_mul_f32_e32 v168, v168, v16\n"
  "v_mul_f32_e32 v169, v169, v16\n"
  "v_mul_f32_e32 v170, v170, v16\n"
  "v_mul_f32_e32 v171, v171, v16\n"
  "v_mul_f32_e32 v172, v172, v16\n"
  "v_mul_f32_e32 v173, v173, v16\n"
  "v_mul_f32_e32 v174, v174, v16\n"
  "v_mul_f32_e32 v175, v175, v16\n"
  "v_mul_f32_e32 v176, v176, v16\n"
  "v_mul_f32_e32 v177, v177, v16\n"
  "v_mul_f32_e32 v178, v178, v16\n"
  "v_mul_f32_e32 v179, v179, v16\n"
  "v_mul_f32_e32 v180, v180, v16\n"
  "v_mul_f32_e32 v181, v181, v16\n"
  "v_mul_f32_e32 v182, v182, v16\n"
  "v_mul_f32_e32 v183, v183, v16\n"
  "v_mul_f32_e32 v184, v184, v16\n"
  "v_mul_f32_e32 v185, v185, v16\n"
  "v_mul_f32_e32 v186, v186, v16\n"
  "v_mul_f32_e32 v187, v187, v16\n"
  "v_mul_f32_e32 v188, v188, v16\n"
  "v_mul_f32_e32 v189, v189, v16\n"
  "v_mul_f32_e32 v190, v190, v16\n"
  "v_mul_f32_e32 v191, v191, v16\n"
  "v_mul_f32_e32 v192, v192, v16\n"
  "v_mul_f32_e32 v193, v193, v16\n"
  "v_mul_f32_e32 v194, v194, v16\n"
  "v_mul_f32_e32 v195, v195, v16\n"
  "v_mul_f32_e32 v196, v196, v16\n"
  "v_mul_f32_e32 v197, v197, v16\n"
  "v_mul_f32_e32 v198, v198, v16\n"
  "v_mul_f32_e32 v199, v199, v16\n"
  "v_mul_f32_e32 v200, v200, v16\n"
  "v_mul_f32_e32 v201, v201, v16\n"
  "v_mul_f32_e32 v202, v202, v16\n"
  "v_mul_f32_e32 v203, v203, v16\n"
  "v_mul_f32_e32 v204, v204, v16\n"
  "v_mul_f32_e32 v205, v205, v16\n"
  "v_mul_f32_e32 v206, v206, v16\n"
  "v_mul_f32_e32 v207, v207, v16\n"
  "v_mul_f32_e32 v11, v11, v17\n"
  "v_mul_f32_e32 v208, v208, v17\n"
  "v_mul_f32_e32 v209, v209, v17\n"
  "v_mul_f32_e32 v210, v210, v17\n"
  "v_mul_f32_e32 v211, v211, v17\n"
  "v_mul_f32_e32 v212, v212, v17\n"
  "v_mul_f32_e32 v213, v213, v17\n"
  "v_mul_f32_e32 v214, v214, v17\n"
  "v_mul_f32_e32 v215, v215, v17\n"
  "v_mul_f32_e32 v216, v216, v17\n"
  "v_mul_f32_e32 v217, v217, v17\n"
  "v_mul_f32_e32 v218, v218, v17\n"
  "v_mul_f32_e32 v219, v219, v17\n"
  "v_mul_f32_e32 v220, v220, v17\n"
  "v_mul_f32_e32 v221, v221, v17\n"
  "v_mul_f32_e32 v222, v222, v17\n"
  "v_mul_f32_e32 v223, v223, v17\n"
  "v_mul_f32_e32 v224, v224, v17\n"
  "v_mul_f32_e32 v225, v225, v17\n"
  "v_mul_f32_e32 v226, v226, v17\n"
  "v_mul_f32_e32 v227, v227, v17\n"
  "v_mul_f32_e32 v228, v228, v17\n"
  "v_mul_f32_e32 v229, v229, v17\n"
  "v_mul_f32_e32 v230, v230, v17\n"
  "v_mul_f32_e32 v231, v231, v17\n"
  "v_mul_f32_e32 v232, v232, v17\n"
  "v_mul_f32_e32 v233, v233, v17\n"
  "v_mul_f32_e32 v234, v234, v17\n"
  "v_mul_f32_e32 v235, v235, v17\n"
  "v_mul_f32_e32 v236, v236, v17\n"
  "v_mul_f32_e32 v237, v237, v17\n"
  "v_mul_f32_e32 v238, v238, v17\n"
  "v_mul_f32_e32 v239, v239, v17\n"
  "v_mul_f32_e32 v240, v240, v17\n"
  "v_mul_f32_e32 v241, v241, v17\n"
  "v_mul_f32_e32 v242, v242, v17\n"
  "v_mul_f32_e32 v243, v243, v17\n"
  "v_mul_f32_e32 v244, v244, v17\n"
  "v_mul_f32_e32 v245, v245, v17\n"
  "v_mul_f32_e32 v246, v246, v17\n"
  "v_mul_f32_e32 v247, v247, v17\n"
  "v_mul_f32_e32 v248, v248, v17\n"
  "v_mul_f32_e32 v249, v249, v17\n"
  "v_mul_f32_e32 v250, v250, v17\n"
  "v_mul_f32_e32 v251, v251, v17\n"
  "v_mul_f32_e32 v252, v252, v17\n"
  "v_mul_f32_e32 v253, v253, v17\n"
  "v_mul_f32_e32 v254, v254, v17\n"
  "v_mul_f32_e32 v255, v255, v17\n"
  "v_exp_f32_e32 v12, v40\n"
  "v_exp_f32_e32 v13, v41\n"
  "v_exp_f32_e32 v14, v42\n"
  "v_exp_f32_e32 v15, v43\n"
  "v_exp_f32_e32 v16, v44\n"
  "v_exp_f32_e32 v17, v45\n"
  "v_exp_f32_e32 v18, v46\n"
  "v_exp_f32_e32 v19, v47\n"
  "v_exp_f32_e32 v20, v48\n"
  "v_exp_f32_e32 v21, v49\n"
  "v_exp_f32_e32 v22, v50\n"
  "v_exp_f32_e32 v23, v51\n"
  "v_exp_f32_e32 v24, v52\n"
  "v_exp_f32_e32 v25, v53\n"
  "v_exp_f32_e32 v26, v54\n"
  "v_exp_f32_e32 v27, v55\n"
  "v_exp_f32_e32 v28, v56\n"
  "v_exp_f32_e32 v29, v57\n"
  "v_exp_f32_e32 v30, v58\n"
  "v_exp_f32_e32 v31, v59\n"
  "v_exp_f32_e32 v32, v60\n"
  "v_exp_f32_e32 v33, v61\n"
  "v_exp_f32_e32 v34, v62\n"
  "v_exp_f32_e32 v35, v63\n"
  "v_add_f32_e32 v36, v12, v13\n"
  "v_add_f32_e32 v37, v20, v21\n"
  "v_add_f32_e32 v38, v28, v29\n"
  "v_add_f32_e32 v36, v36, v14\n"
  "v_add_f32_e32 v37, v37, v22\n"
  "v_add_f32_e32 v38, v38, v30\n"
  "v_add_f32_e32 v36, v36, v15\n"
  "v_add_f32_e32 v37, v37, v23\n"
  "v_add_f32_e32 v38, v38, v31\n"
  "v_add_f32_e32 v36, v36, v16\n"
  "v_add_f32_e32 v37, v37, v24\n"
  "v_add_f32_e32 v38, v38, v32\n"
  "v_add_f32_e32 v36, v36, v17\n"
  "v_add_f32_e32 v37, v37, v25\n"
  "v_add_f32_e32 v38, v38, v33\n"
  "v_add_f32_e32 v36, v36, v18\n"
  "v_add_f32_e32 v37, v37, v26\n"
  "v_add_f32_e32 v38, v38, v34\n"
  "v_add_f32_e32 v36, v36, v19\n"
  "v_add_f32_e32 v37, v37, v27\n"
  "v_add_f32_e32 v38, v38, v35\n"
  "s_and_b32 s63, s50, 15\n"
  "s_cmp_eq_u32 s63, 15\n"
  "s_cbranch_scc1 .LH_slow_ret15a\n"
  "s_branch .LH_slow_reta\n"
  ".LH_w7:\n"
  "s_cmp_lt_u32 s59, 4\n"
  "s_cbranch_scc0 .LH_pBb\n"
  "s_add_u32 m0, s49, 0\n"
  "s_nop 0\n"
  "buffer_load_dwordx4 v4, s[36:39], s48 offen offset:0 nt lds\n"
  "s_add_u32 m0, s49, 3968\n"
  "s_nop 0\n"
  "buffer_load_dwordx4 v4, s[36:39], s48 offen offset:128 nt lds\n"
  "s_add_u32 m0, s49, 7936\n"
  "s_nop 0\n"
  "buffer_load_dwordx4 v4, s[36:39], s48 offen offset:256 nt lds\n"
  "s_add_u32 s49, s49, s60\n"
  "s_add_u32 s48, s48, s60\n"
  "buffer_load_dwordx4 v[64:67], v4, s[40:43], s72 offen offset:0 nt\n"
  "buffer_load_dwordx4 v[68:71], v4, s[40:43], s72 offen offset:3072 nt\n"
  "buffer_load_dwordx4 v[88:91], v4, s[40:43], s77 offen offset:0 nt\n"
  "buffer_load_dwordx4 v[92:95], v4, s[40:43], s77 offen offset:3072 nt\n"
  "buffer_load_dwordx4 v[72:75], v4, s[40:43], s72 offen offset:128 nt\n"
  "buffer_load_dwordx4 v[76:79], v4, s[40:43], s72 offen offset:3200 nt\n"
  "buffer_load_dwordx4 v[96:99], v4, s[40:43], s77 offen offset:128 nt\n"
  "buffer_load_dwordx4 v[100:103], v4, s[40:43], s77 offen offset:3200 nt\n"
  "buffer_load_dwordx4 v[80:83], v4, s[40:43], s72 offen offset:256 nt\n"
  "buffer_load_dwordx4 v[84:87], v4, s[40:43], s72 offen offset:3328 nt\n"
  "buffer_load_dwordx4 v[104:107], v4, s[40:43], s77 offen offset:256 nt\n"
  "buffer_load_dwordx4 v[108:111], v4, s[40:43], s77 offen offset:3328 nt\n"
  "s_branch .LH_pCb\n"
  ".LH_pBb:\n"
  "buffer_load_dwordx4 v[64:67], v4, s[40:43], s72 offen offset:0 nt\n"
  "buffer_load_dwordx4 v[68:71], v4, s[40:43], s72 offen offset:3072 nt\n"
  "buffer_load_dwordx4 v[88:91], v4, s[40:43], s77 offen offset:0 nt\n"
  "buffer_load_dwordx4 v[92:95], v4, s[40:43], s77 offen offset:3072 nt\n"
  "buffer_load_dwordx4 v[72:75], v4, s[40:43], s72 offen offset:128 nt\n"
  "buffer_load_dwordx4 v[76:79], v4, s[40:43], s72 offen offset:3200 nt\n"
  "buffer_load_dwordx4 v[96:99], v4, s[40:43], s77 offen offset:128 nt\n"
  "buffer_load_dwordx4 v[100:103], v4, s[40:43], s77 offen offset:3200 nt\n"
  "buffer_load_dwordx4 v[80:83], v4, s[40:43], s72 offen offset:256 nt\n"
  "buffer_load_dwordx4 v[84:87], v4, s[40:43], s72 offen offset:3328 nt\n"
  "buffer_load_dwordx4 v[104:107], v4, s[40:43], s77 offen offset:256 nt\n"
  "buffer_load_dwordx4 v[108:111], v4, s[40:43], s77 offen offset:3328 nt\n"
  "s_add_u32 m0, s49, 0\n"
  "s_nop 0\n"
  "buffer_load_dwordx4 v4, s[36:39], s48 offen offset:0 nt lds\n"
  "s_add_u32 m0, s49, 3968\n"
  "s_nop 0\n"
  "buffer_load_dwordx4 v4, s[36:39], s48 offen offset:128 nt lds\n"
  "s_add_u32 m0, s49, 7936\n"
  "s_nop 0\n"
  "buffer_load_dwordx4 v4, s[36:39], s48 offen offset:256 nt lds\n"
  "s_add_u32 s49, s49, s60\n"
  "s_add_u32 s48, s48, s60\n"
  ".LH_pCb:\n"
  "s_cmp_lt_u32 s59, 4\n"
  "s_cbranch_scc1 .LH_startb\n"
  "s_barrier\n"
  ".LH_startb:\n"
  "v_mbcnt_lo_u32_b32 v38, -1, 0\n"
  "v_mbcnt_hi_u32_b32 v38, -1, v38\n"
  "v_lshlrev_b32_e32 v38, 4, v38\n"
  "v_add_u32_e32 v38, s58, v38\n"
  "s_sub_u32 s62, s58, s57\n"
  "v_add_u32_e32 v39, s62, v3\n"
  "s_waitcnt vmcnt(0)\n"
  "s_add_u32 m0, s49, 0\n"
  "s_nop 0\n"
  "buffer_load_dwordx4 v4, s[36:39], s48 offen offset:0 nt lds\n"
  "s_add_u32 m0, s49, 3968\n"
  "s_nop 0\n"
  "buffer_load_dwordx4 v4, s[36:39], s48 offen offset:128 nt lds\n"
  "s_add_u32 m0, s49, 7936\n"
  "s_nop 0\n"
  "buffer_load_dwordx4 v4, s[36:39], s48 offen offset:256 nt lds\n"
  "s_add_u32 s49, s49, s60\n"
  "s_add_u32 s48, s48, s60\n"
  "s_add_u32 m0, s49, 0\n"
  "s_nop 0\n"
  "buffer_load_dwordx4 v4, s[36:39], s48 offen offset:0 nt lds\n"
  "s_add_u32 m0, s49, 3968\n"
  "s_nop 0\n"
  "buffer_load_dwordx4 v4, s[36:39], s48 offen offset:128 nt lds\n"
  "s_add_u32 m0, s49, 7936\n"
  "s_nop 0\n"
  "buffer_load_dwordx4 v4, s[36:39], s48 offen offset:256 nt lds\n"
  "s_add_u32 s49, s49, s60\n"
  "s_add_u32 s48, s48, s60\n"
  "ds_write_b128 v38, v[64:67]\n"
  "ds_write_b128 v38, v[68:71] offset:1024\n"
  "ds_read_b128 v[64:67], v39\n"
  "ds_read_b128 v[68:71], v39 offset:512\n"
  "ds_write_b128 v38, v[88:91]\n"
  "ds_write_b128 v38, v[92:95] offset:1024\n"
  "ds_read_b128 v[88:91], v39\n"
  "ds_read_b128 v[92:95], v39 offset:512\n"
  "ds_write_b128 v38, v[72:75]\n"
  "ds_write_b128 v38, v[76:79] offset:1024\n"
  "ds_read_b128 v[72:75], v39\n"
  "ds_read_b128 v[76:79], v39 offset:512\n"
  "ds_write_b128 v38, v[96:99]\n"
  "ds_write_b128 v38, v[100:103] offset:1024\n"
  "ds_read_b128 v[96:99], v39\n"
  "ds_read_b128 v[100:103], v39 offset:512\n"
  "ds_write_b128 v38, v[80:83]\n"
  "ds_write_b128 v38, v[84:87] offset:1024\n"
  "ds_read_b128 v[80:83], v39\n"
  "ds_read_b128 v[84:87], v39 offset:512\n"
  "ds_write_b128 v38, v[104:107]\n"
  "ds_write_b128 v38, v[108:111] offset:1024\n"
  "ds_read_b128 v[104:107], v39\n"
  "ds_read_b128 v[108:111], v39 offset:512\n"
  "s_barrier\n"
  "ds_read_b128 v[12:15], v0 offset:0\n"
  "ds_read_b128 v[16:19], v0 offset:512\n"
  "ds_read_b128 v[20:23], v0 offset:2048\n"
  "ds_read_b128 v[24:27], v0 offset:2560\n"
  "ds_read_b128 v[28:31], v0 offset:4096\n"
  "ds_read_b128 v[32:35], v0 offset:4608\n"
  "ds_read_b128 v[36:39], v0 offset:6144\n"
  "s_waitcnt lgkmcnt(3)\n"
  "v_mfma_f32_16x16x32_bf16 v[40:43], v[12:15], v[64:67], 0\n"
  "v_mfma_f32_16x16x32_bf16 v[40:43], v[16:19], v[68:71], v[40:43]\n"
  "v_mfma_f32_16x16x32_bf16 v[48:51], v[12:15], v[88:91], 0\n"
  "ds_read_b128 v[12:15], v0 offset:6656\n"
  "v_mfma_f32_16x16x32_bf16 v[48:51], v[16:19], v[92:95], v[48:51]\n"
  "ds_read_b128 v[16:19], v0 offset:8192\n"
  "v_mfma_f32_16x16x32_bf16 v[52:55], v[20:23], v[88:91], 0\n"
  "v_mfma_f32_16x16x32_bf16 v[52:55], v[24:27], v[92:95], v[52:55]\n"
  "v_mfma_f32_16x16x32_bf16 v[44:47], v[20:23], v[64:67], 0\n"
  "ds_read_b128 v[20:23], v0 offset:8704\n"
  "v_mfma_f32_16x16x32_bf16 v[44:47], v[24:27], v[68:71], v[44:47]\n"
  "ds_read_b128 v[24:27], v0 offset:10240\n"
  "s_waitcnt lgkmcnt(3)\n"
  "v_mfma_f32_16x16x32_bf16 v[40:43], v[28:31], v[72:75], v[40:43]\n"
  "v_mfma_f32_16x16x32_bf16 v[40:43], v[32:35], v[76:79], v[40:43]\n"
  "v_mfma_f32_16x16x32_bf16 v[48:51], v[28:31], v[96:99], v[48:51]\n"
  "ds_read_b128 v[28:31], v0 offset:10752\n"
  "v_mfma_f32_16x16x32_bf16 v[48:51], v[32:35], v[100:103], v[48:51]\n"
  "v_mfma_f32_16x16x32_bf16 v[52:55], v[36:39], v[96:99], v[52:55]\n"
  "v_mfma_f32_16x16x32_bf16 v[52:55], v[12:15], v[100:103], v[52:55]\n"
  "v_mfma_f32_16x16x32_bf16 v[44:47], v[36:39], v[72:75], v[44:47]\n"
  "v_mfma_f32_16x16x32_bf16 v[44:47], v[12:15], v[76:79], v[44:47]\n"
  "s_waitcnt lgkmcnt(0)\n"
  "v_mfma_f32_16x16x32_bf16 v[40:43], v[16:19], v[80:83], v[40:43]\n"
  "v_mfma_f32_16x16x32_bf16 v[40:43], v[20:23], v[84:87], v[40:43]\n"
  "v_mfma_f32_16x16x32_bf16 v[48:51], v[16:19], v[104:107], v[48:51]\n"
  "v_mfma_f32_16x16x32_bf16 v[48:51], v[20:23], v[108:111], v[48:51]\n"
  "v_mfma_f32_16x16x32_bf16 v[52:55], v[24:27], v[104:107], v[52:55]\n"
  "v_mfma_f32_16x16x32_bf16 v[52:55], v[28:31], v[108:111], v[52:55]\n"
  "v_mfma_f32_16x16x32_bf16 v[44:47], v[24:27], v[80:83], v[44:47]\n"
  "v_mfma_f32_16x16x32_bf16 v[44:47], v[28:31], v[84:87], v[44:47]\n"
  "s_barrier\n"
  "s_add_u32 m0, s49, 0\n"
  "s_nop 0\n"
  "buffer_load_dwordx4 v4, s[36:39], s48 offen offset:0 nt lds\n"
  "s_add_u32 m0, s49, 3968\n"
  "s_nop 0\n"
  "buffer_load_dwordx4 v4, s[36:39], s48 offen offset:128 nt lds\n"
  "s_add_u32 m0, s49, 7936\n"
  "s_nop 0\n"
  "buffer_load_dwordx4 v4, s[36:39], s48 offen offset:256 nt lds\n"
  "s_and_b32 s62, s50, 3\n"
  "s_cmp_eq_u32 s62, 3\n"
  "s_cselect_b32 s54, s61, s60\n"
  "s_cmp_eq_u32 s62, 0\n"
  "s_cselect_b32 s55, s61, s60\n"
  "s_add_u32 s49, s49, s55\n"
  "s_add_u32 s48, s48, s60\n"
  "v_add_u32_e32 v0, s54, v0\n"
  "v_add_u32_e32 v1, s55, v1\n"
  "v_add_u32_e32 v2, s55, v2\n"
  "v_max3_f32 v12, v40, v41, v42\n"
  "v_max3_f32 v13, v48, v49, v50\n"
  "v_max3_f32 v12, v12, v43, v44\n"
  "v_max3_f32 v13, v13, v51, v52\n"
  "v_max3_f32 v12, v12, v45, v46\n"
  "v_max3_f32 v13, v13, v53, v54\n"
  "v_max_f32_e32 v12, v12, v47\n"
  "v_max_f32_e32 v13, v13, v55\n"
  "v_mov_b32_e32 v15, v12\n"
  "v_mov_b32_e32 v16, v13\n"
  "s_nop 1\n"
  "v_permlane32_swap_b32_e32 v12, v15\n"
  "v_permlane32_swap_b32_e32 v13, v16\n"
  "s_nop 1\n"
  "v_max_f32_e32 v12, v12, v15\n"
  "v_max_f32_e32 v13, v13, v16\n"
  "v_mov_b32_e32 v15, v12\n"
  "v_mov_b32_e32 v16, v13\n"
  "s_nop 1\n"
  "v_permlane16_swap_b32_e32 v12, v15\n"
  "v_permlane16_swap_b32_e32 v13, v16\n"
  "s_nop 1\n"
  "v_max_f32_e32 v12, v12, v15\n"
  "v_max_f32_e32 v13, v13, v16\n"
  "v_mul_f32_e64 v6, -v12, s52\n"
  "v_mul_f32_e64 v8, -v13, s52\n"
  "v_fma_f32 v40, v40, s52, v6\n"
  "v_fma_f32 v41, v41, s52, v6\n"
  "v_fma_f32 v42, v42, s52, v6\n"
  "v_fma_f32 v43, v43, s52, v6\n"
  "v_fma_f32 v48, v48, s52, v8\n"
  "v_fma_f32 v49, v49, s52, v8\n"
  "v_fma_f32 v50, v50, s52, v8\n"
  "v_fma_f32 v51, v51, s52, v8\n"
  "v_fma_f32 v52, v52, s52, v8\n"
  "v_fma_f32 v53, v53, s52, v8\n"
  "v_fma_f32 v54, v54, s52, v8\n"
  "v_fma_f32 v55, v55, s52, v8\n"
  "v_fma_f32 v44, v44, s52, v6\n"
  "v_fma_f32 v45, v45, s52, v6\n"
  "v_fma_f32 v46, v46, s52, v6\n"
  "v_fma_f32 v47, v47, s52, v6\n"
  "v_exp_f32_e32 v40, v40\n"
  "v_exp_f32_e32 v41, v41\n"
  "v_exp_f32_e32 v42, v42\n"
  "v_exp_f32_e32 v43, v43\n"
  "v_exp_f32_e32 v44, v44\n"
  "v_exp_f32_e32 v45, v45\n"
  "v_exp_f32_e32 v46, v46\n"
  "v_exp_f32_e32 v47, v47\n"
  "v_exp_f32_e32 v48, v48\n"
  "v_exp_f32_e32 v49, v49\n"
  "v_exp_f32_e32 v50, v50\n"
  "v_exp_f32_e32 v51, v51\n"
  "v_exp_f32_e32 v52, v52\n"
  "v_exp_f32_e32 v53, v53\n"
  "v_exp_f32_e32 v54, v54\n"
  "v_exp_f32_e32 v55, v55\n"
  "v_add_f32_e32 v7, v40, v41\n"
  "v_add_f32_e32 v9, v48, v49\n"
  "v_add_f32_e32 v7, v7, v42\n"
  "v_add_f32_e32 v9, v9, v50\n"
  "v_add_f32_e32 v7, v7, v43\n"
  "v_add_f32_e32 v9, v9, v51\n"
  "v_add_f32_e32 v7, v7, v44\n"
  "v_add_f32_e32 v9, v9, v52\n"
  "v_add_f32_e32 v7, v7, v45\n"
  "v_add_f32_e32 v9, v9, v53\n"
  "v_add_f32_e32 v7, v7, v46\n"
  "v_add_f32_e32 v9, v9, v54\n"
  "v_add_f32_e32 v7, v7, v47\n"
  "v_add_f32_e32 v9, v9, v55\n"
  "v_cvt_pk_bf16_f32 v40, v40, v41\n"
  "v_cvt_pk_bf16_f32 v41, v42, v43\n"
  "v_cvt_pk_bf16_f32 v42, v44, v45\n"
  "v_cvt_pk_bf16_f32 v43, v46, v47\n"
  "v_cvt_pk_bf16_f32 v48, v48, v49\n"
  "v_cvt_pk_bf16_f32 v49, v50, v51\n"
  "v_cvt_pk_bf16_f32 v50, v52, v53\n"
  "v_cvt_pk_bf16_f32 v51, v54, v55\n"
  "ds_read_b64_tr_b16 v[12:13], v1 offset:0\n"
  "ds_read_b64_tr_b16 v[14:15], v1 offset:2048\n"
  "ds_read_b64_tr_b16 v[16:17], v2 offset:0\n"
  "ds_read_b64_tr_b16 v[18:19], v2 offset:2048\n"
  "ds_read_b64_tr_b16 v[20:21], v1 offset:512\n"
  "ds_read_b64_tr_b16 v[22:23], v1 offset:2560\n"
  "ds_read_b64_tr_b16 v[24:25], v2 offset:512\n"
  "ds_read_b64_tr_b16 v[26:27], v2 offset:2560\n"
  "s_waitcnt vmcnt(6)\n"
  "s_barrier\n"
  "s_waitcnt lgkmcnt(2)\n"
  "v_mfma_f32_16x16x32_bf16 v[112:115], v[12:15], v[40:43], 0\n"
  "ds_read_b64_tr_b16 v[28:29], v1 offset:4096\n"
  "v_mfma_f32_16x16x32_bf16 v[160:163], v[12:15], v[48:51], 0\n"
  "ds_read_b64_tr_b16 v[30:31], v1 offset:6144\n"
  "v_mfma_f32_16x16x32_bf16 v[164:167], v[16:19], v[48:51], 0\n"
  "ds_read_b64_tr_b16 v[32:33], v2 offset:4096\n"
  "v_mfma_f32_16x16x32_bf16 v[116:119], v[16:19], v[40:43], 0\n"
  "ds_read_b64_tr_b16 v[34:35], v2 offset:6144\n"
  "v_mfma_f32_16x16x32_bf16 v[120:123], v[20:23], v[40:43], 0\n"
  "ds_read_b64_tr_b16 v[36:37], v1 offset:4608\n"
  "v_mfma_f32_16x16x32_bf16 v[168:171], v[20:23], v[48:51], 0\n"
  "ds_read_b64_tr_b16 v[38:39], v1 offset:6656\n"
  "s_waitcnt lgkmcnt(2)\n"
  "v_mfma_f32_16x16x32_bf16 v[172:175], v[24:27], v[48:51], 0\n"
  "ds_read_b64_tr_b16 v[12:13], v2 offset:4608\n"
  "v_mfma_f32_16x16x32_bf16 v[124:127], v[24:27], v[40:43], 0\n"
  "ds_read_b64_tr_b16 v[14:15], v2 offset:6656\n"
  "v_mfma_f32_16x16x32_bf16 v[128:131], v[28:31], v[40:43], 0\n"
  "ds_read_b64_tr_b16 v[16:17], v1 offset:8192\n"
  "v_mfma_f32_16x16x32_bf16 v[176:179], v[28:31], v[48:51], 0\n"
  "ds_read_b64_tr_b16 v[18:19], v1 offset:10240\n"
  "v_mfma_f32_16x16x32_bf16 v[180:183], v[32:35], v[48:51], 0\n"
  "ds_read_b64_tr_b16 v[20:21], v2 offset:8192\n"
  "v_mfma_f32_16x16x32_bf16 v[132:135], v[32:35], v[40:43], 0\n"
  "ds_read_b64_tr_b16 v[22:23], v2 offset:10240\n"
  "s_waitcnt lgkmcnt(2)\n"
  "v_mfma_f32_16x16x32_bf16 v[136:139], v[36:39], v[40:43], 0\n"
  "ds_read_b64_tr_b16 v[24:25], v1 offset:8704\n"
  "v_mfma_f32_16x16x32_bf16 v[184:187], v[36:39], v[48:51], 0\n"
  "ds_read_b64_tr_b16 v[26:27], v1 offset:10752\n"
  "v_mfma_f32_16x16x32_bf16 v[188:191], v[12:15], v[48:51], 0\n"
  "ds_read_b64_tr_b16 v[28:29], v2 offset:8704\n"
  "v_mfma_f32_16x16x32_bf16 v[140:143], v[12:15], v[40:43], 0\n"
  "ds_read_b64_tr_b16 v[30:31], v2 offset:10752\n"
  "v_mfma_f32_16x16x32_bf16 v[144:147], v[16:19], v[40:43], 0\n"
  "ds_read_b128 v[32:35], v0 offset:0\n"
  "v_mfma_f32_16x16x32_bf16 v[192:195], v[16:19], v[48:51], 0\n"
  "ds_read_b128 v[36:39], v0 offset:512\n"
  "s_waitcnt lgkmcnt(2)\n"
  "v_mfma_f32_16x16x32_bf16 v[196:199], v[20:23], v[48:51], 0\n"
  "ds_read_b128 v[12:15], v0 offset:2048\n"
  "v_mfma_f32_16x16x32_bf16 v[148:151], v[20:23], v[40:43], 0\n"
  "ds_read_b128 v[16:19], v0 offset:2560\n"
  "v_mfma_f32_16x16x32_bf16 v[152:155], v[24:27], v[40:43], 0\n"
  "ds_read_b128 v[20:23], v0 offset:4096\n"
  "v_mfma_f32_16x16x32_bf16 v[200:203], v[24:27], v[48:51], 0\n"
  "ds_read_b128 v[24:27], v0 offset:4608\n"
  "v_mfma_f32_16x16x32_bf16 v[204:207], v[28:31], v[48:51], 0\n"
  "v_mfma_f32_16x16x32_bf16 v[156:159], v[28:31], v[40:43], 0\n"
  "ds_read_b128 v[28:31], v0 offset:6144\n"
  "s_waitcnt lgkmcnt(3)\n"
  "v_mfma_f32_16x16x32_bf16 v[40:43], v[32:35], v[64:67], 0\n"
  "v_mfma_f32_16x16x32_bf16 v[40:43], v[36:39], v[68:71], v[40:43]\n"
  "v_mfma_f32_16x16x32_bf16 v[48:51], v[32:35], v[88:91], 0\n"
  "ds_read_b128 v[32:35], v0 offset:6656\n"
  "v_mfma_f32_16x16x32_bf16 v[48:51], v[36:39], v[92:95], v[48:51]\n"
  "ds_read_b128 v[36:39], v0 offset:8192\n"
  "v_mfma_f32_16x16x32_bf16 v[52:55], v[12:15], v[88:91], 0\n"
  "v_mfma_f32_16x16x32_bf16 v[52:55], v[16:19], v[92:95], v[52:55]\n"
  "v_mfma_f32_16x16x32_bf16 v[44:47], v[12:15], v[64:67], 0\n"
  "ds_read_b128 v[12:15], v0 offset:8704\n"
  "v_mfma_f32_16x16x32_bf16 v[44:47], v[16:19], v[68:71], v[44:47]\n"
  "ds_read_b128 v[16:19], v0 offset:10240\n"
  "s_waitcnt lgkmcnt(3)\n"
  "v_mfma_f32_16x16x32_bf16 v[40:43], v[20:23], v[72:75], v[40:43]\n"
  "v_mfma_f32_16x16x32_bf16 v[40:43], v[24:27], v[76:79], v[40:43]\n"
  "v_mfma_f32_16x16x32_bf16 v[48:51], v[20:23], v[96:99], v[48:51]\n"
  "ds_read_b128 v[20:23], v0 offset:10752\n"
  "v_mfma_f32_16x16x32_bf16 v[48:51], v[24:27], v[100:103], v[48:51]\n"
  "v_mfma_f32_16x16x32_bf16 v[52:55], v[28:31], v[96:99], v[52:55]\n"
  "v_mfma_f32_16x16x32_bf16 v[52:55], v[32:35], v[100:103], v[52:55]\n"
  "v_mfma_f32_16x16x32_bf16 v[44:47], v[28:31], v[72:75], v[44:47]\n"
  "v_mfma_f32_16x16x32_bf16 v[44:47], v[32:35], v[76:79], v[44:47]\n"
  "s_waitcnt lgkmcnt(0)\n"
  "v_mfma_f32_16x16x32_bf16 v[40:43], v[36:39], v[80:83], v[40:43]\n"
  "v_mfma_f32_16x16x32_bf16 v[40:43], v[12:15], v[84:87], v[40:43]\n"
  "v_mfma_f32_16x16x32_bf16 v[48:51], v[36:39], v[104:107], v[48:51]\n"
  "v_mfma_f32_16x16x32_bf16 v[48:51], v[12:15], v[108:111], v[48:51]\n"
  "v_mfma_f32_16x16x32_bf16 v[52:55], v[16:19], v[104:107], v[52:55]\n"
  "v_mfma_f32_16x16x32_bf16 v[52:55], v[20:23], v[108:111], v[52:55]\n"
  "v_mfma_f32_16x16x32_bf16 v[44:47], v[16:19], v[80:83], v[44:47]\n"
  "v_mfma_f32_16x16x32_bf16 v[44:47], v[20:23], v[84:87], v[44:47]\n"
  "s_mov_b32 s50, 1\n"
  ".LH_loopb:\n"
  "s_barrier\n"
  "s_nop 5\n"
  "v_fma_f32 v40, v40, s52, v6\n"
  "v_fma_f32 v41, v41, s52, v6\n"
  "s_add_u32 m0, s49, 0\n"
  "v_fma_f32 v42, v42, s52, v6\n"
  "v_fma_f32 v43, v43, s52, v6\n"
  "buffer_load_dwordx4 v4, s[36:39], s48 offen offset:0 nt lds\n"
  "s_add_u32 m0, s49, 3968\n"
  "v_fma_f32 v48, v48, s52, v8\n"
  "v_fma_f32 v49, v49, s52, v8\n"
  "buffer_load_dwordx4 v4, s[36:39], s48 offen offset:128 nt lds\n"
  "s_add_u32 m0, s49, 7936\n"
  "v_fma_f32 v50, v50, s52, v8\n"
  "v_fma_f32 v51, v51, s52, v8\n"
  "buffer_load_dwordx4 v4, s[36:39], s48 offen offset:256 nt lds\n"
  "s_and_b32 s62, s50, 3\n"
  "v_fma_f32 v52, v52, s52, v8\n"
  "v_fma_f32 v53, v53, s52, v8\n"
  "s_cmp_eq_u32 s62, 3\n"
  "s_cselect_b32 s54, s61, s60\n"
  "v_fma_f32 v54, v54, s52, v8\n"
  "v_fma_f32 v55, v55, s52, v8\n"
  "s_cmp_eq_u32 s62, 0\n"
  "s_cselect_b32 s55, s61, s60\n"
  "v_fma_f32 v44, v44, s52, v6\n"
  "v_fma_f32 v45, v45, s52, v6\n"
  "s_add_u32 s49, s49, s55\n"
  "s_add_u32 s48, s48, s60\n"
  "v_fma_f32 v46, v46, s52, v6\n"
  "v_fma_f32 v47, v47, s52, v6\n"
  "v_exp_f32_e32 v12, v40\n"
  "v_exp_f32_e32 v13, v41\n"
  "v_exp_f32_e32 v14, v42\n"
  "v_exp_f32_e32 v15, v43\n"
  "v_exp_f32_e32 v16, v44\n"
  "v_exp_f32_e32 v17, v45\n"
  "v_exp_f32_e32 v18, v46\n"
  "v_exp_f32_e32 v19, v47\n"
  "v_exp_f32_e32 v20, v48\n"
  "v_exp_f32_e32 v21, v49\n"
  "v_exp_f32_e32 v22, v50\n"
  "v_exp_f32_e32 v23, v51\n"
  "v_exp_f32_e32 v24, v52\n"
  "v_exp_f32_e32 v25, v53\n"
  "v_exp_f32_e32 v26, v54\n"
  "v_exp_f32_e32 v27, v55\n"
  "v_add_f32_e32 v36, v12, v13\n"
  "v_add_f32_e32 v37, v20, v21\n"
  "v_add_f32_e32 v36, v36, v14\n"
  "v_add_f32_e32 v37, v37, v22\n"
  "v_add_f32_e32 v36, v36, v15\n"
  "v_add_f32_e32 v37, v37, v23\n"
  "v_add_f32_e32 v36, v36, v16\n"
  "v_add_f32_e32 v37, v37, v24\n"
  "v_add_f32_e32 v36, v36, v17\n"
  "v_add_f32_e32 v37, v37, v25\n"
  "v_add_f32_e32 v36, v36, v18\n"
  "v_add_f32_e32 v37, v37, v26\n"
  "v_add_f32_e32 v36, v36, v19\n"
  "v_add_f32_e32 v37, v37, v27\n"
  "v_max_f32_e32 v39, v36, v37\n"
  "v_cmp_lt_f32_e32 vcc, s53, v39\n"
  "v_add_u32_e32 v0, s54, v0\n"
  "v_add_u32_e32 v1, s55, v1\n"
  "v_add_u32_e32 v2, s55, v2\n"
  "s_cbranch_vccnz .LH_slowb\n"
  ".LH_slow_retb:\n"
  "v_add_f32_e32 v7, v7, v36\n"
  "v_add_f32_e32 v9, v9, v37\n"
  "v_cvt_pk_bf16_f32 v40, v12, v13\n"
  "v_cvt_pk_bf16_f32 v41, v14, v15\n"
  "v_cvt_pk_bf16_f32 v42, v16, v17\n"
  "v_cvt_pk_bf16_f32 v43, v18, v19\n"
  "v_cvt_pk_bf16_f32 v48, v20, v21\n"
  "v_cvt_pk_bf16_f32 v49, v22, v23\n"
  "v_cvt_pk_bf16_f32 v50, v24, v25\n"
  "v_cvt_pk_bf16_f32 v51, v26, v27\n"
  "ds_read_b64_tr_b16 v[12:13], v1 offset:0\n"
  "ds_read_b64_tr_b16 v[14:15], v1 offset:2048\n"
  "ds_read_b64_tr_b16 v[16:17], v2 offset:0\n"
  "ds_read_b64_tr_b16 v[18:19], v2 offset:2048\n"
  "ds_read_b64_tr_b16 v[20:21], v1 offset:512\n"
  "ds_read_b64_tr_b16 v[22:23], v1 offset:2560\n"
  "ds_read_b64_tr_b16 v[24:25], v2 offset:512\n"
  "ds_read_b64_tr_b16 v[26:27], v2 offset:2560\n"
  "s_bitcmp1_b32 s83, s50\n"
  "s_cbranch_scc1 .LH_specialb\n"
  "s_waitcnt vmcnt(6)\n"
  ".LH_midb:\n"
  "s_barrier\n"
  "s_waitcnt lgkmcnt(2)\n"
  "v_mfma_f32_16x16x32_bf16 v[112:115], v[12:15], v[40:43], v[112:115]\n"
  "ds_read_b64_tr_b16 v[28:29], v1 offset:4096\n"
  "v_mfma_f32_16x16x32_bf16 v[160:163], v[12:15], v[48:51], v[160:163]\n"
  "ds_read_b64_tr_b16 v[30:31], v1 offset:6144\n"
  "v_mfma_f32_16x16x32_bf16 v[164:167], v[16:19], v[48:51], v[164:167]\n"
  "ds_read_b64_tr_b16 v[32:33], v2 offset:4096\n"
  "v_mfma_f32_16x16x32_bf16 v[116:119], v[16:19], v[40:43], v[116:119]\n"
  "ds_read_b64_tr_b16 v[34:35], v2 offset:6144\n"
  "v_mfma_f32_16x16x32_bf16 v[120:123], v[20:23], v[40:43], v[120:123]\n"
  "ds_read_b64_tr_b16 v[36:37], v1 offset:4608\n"
  "v_mfma_f32_16x16x32_bf16 v[168:171], v[20:23], v[48:51], v[168:171]\n"
  "ds_read_b64_tr_b16 v[38:39], v1 offset:6656\n"
  "s_waitcnt lgkmcnt(2)\n"
  "v_mfma_f32_16x16x32_bf16 v[172:175], v[24:27], v[48:51], v[172:175]\n"
  "ds_read_b64_tr_b16 v[12:13], v2 offset:4608\n"
  "v_mfma_f32_16x16x32_bf16 v[124:127], v[24:27], v[40:43], v[124:127]\n"
  "ds_read_b64_tr_b16 v[14:15], v2 offset:6656\n"
  "v_mfma_f32_16x16x32_bf16 v[128:131], v[28:31], v[40:43], v[128:131]\n"
  "ds_read_b64_tr_b16 v[16:17], v1 offset:8192\n"
  "v_mfma_f32_16x16x32_bf16 v[176:179], v[28:31], v[48:51], v[176:179]\n"
  "ds_read_b64_tr_b16 v[18:19], v1 offset:10240\n"
  "v_mfma_f32_16x16x32_bf16 v[180:183], v[32:35], v[48:51], v[180:183]\n"
  "ds_read_b64_tr_b16 v[20:21], v2 offset:8192\n"
  "v_mfma_f32_16x16x32_bf16 v[132:135], v[32:35], v[40:43], v[132:135]\n"
  "ds_read_b64_tr_b16 v[22:23], v2 offset:10240\n"
  "s_waitcnt lgkmcnt(2)\n"
  "v_mfma_f32_16x16x32_bf16 v[136:139], v[36:39], v[40:43], v[136:139]\n"
  "ds_read_b64_tr_b16 v[24:25], v1 offset:8704\n"
  "v_mfma_f32_16x16x32_bf16 v[184:187], v[36:39], v[48:51], v[184:187]\n"
  "ds_read_b64_tr_b16 v[26:27], v1 offset:10752\n"
  "v_mfma_f32_16x16x32_bf16 v[188:191], v[12:15], v[48:51], v[188:191]\n"
  "ds_read_b64_tr_b16 v[28:29], v2 offset:8704\n"
  "v_mfma_f32_16x16x32_bf16 v[140:143], v[12:15], v[40:43], v[140:143]\n"
  "ds_read_b64_tr_b16 v[30:31], v2 offset:10752\n"
  "v_mfma_f32_16x16x32_bf16 v[144:147], v[16:19], v[40:43], v[144:147]\n"
  "ds_read_b128 v[32:35], v0 offset:0\n"
  "v_mfma_f32_16x16x32_bf16 v[192:195], v[16:19], v[48:51], v[192:195]\n"
  "ds_read_b128 v[36:39], v0 offset:512\n"
  "s_waitcnt lgkmcnt(2)\n"
  "v_mfma_f32_16x16x32_bf16 v[196:199], v[20:23], v[48:51], v[196:199]\n"
  "ds_read_b128 v[12:15], v0 offset:2048\n"
  "v_mfma_f32_16x16x32_bf16 v[148:151], v[20:23], v[40:43], v[148:151]\n"
  "ds_read_b128 v[16:19], v0 offset:2560\n"
  "v_mfma_f32_16x16x32_bf16 v[152:155], v[24:27], v[40:43], v[152:155]\n"
  "ds_read_b128 v[20:23], v0 offset:4096\n"
  "v_mfma_f32_16x16x32_bf16 v[200:203], v[24:27], v[48:51], v[200:203]\n"
  "ds_read_b128 v[24:27], v0 offset:4608\n"
  "v_mfma_f32_16x16x32_bf16 v[204:207], v[28:31], v[48:51], v[204:207]\n"
  "v_mfma_f32_16x16x32_bf16 v[156:159], v[28:31], v[40:43], v[156:159]\n"
  "ds_read_b128 v[28:31], v0 offset:6144\n"
  "s_waitcnt lgkmcnt(3)\n"
  "v_mfma_f32_16x16x32_bf16 v[40:43], v[32:35], v[64:67], 0\n"
  "v_mfma_f32_16x16x32_bf16 v[40:43], v[36:39], v[68:71], v[40:43]\n"
  "v_mfma_f32_16x16x32_bf16 v[48:51], v[32:35], v[88:91], 0\n"
  "ds_read_b128 v[32:35], v0 offset:6656\n"
  "v_mfma_f32_16x16x32_bf16 v[48:51], v[36:39], v[92:95], v[48:51]\n"
  "ds_read_b128 v[36:39], v0 offset:8192\n"
  "v_mfma_f32_16x16x32_bf16 v[52:55], v[12:15], v[88:91], 0\n"
  "v_mfma_f32_16x16x32_bf16 v[52:55], v[16:19], v[92:95], v[52:55]\n"
  "v_mfma_f32_16x16x32_bf16 v[44:47], v[12:15], v[64:67], 0\n"
  "ds_read_b128 v[12:15], v0 offset:8704\n"
  "v_mfma_f32_16x16x32_bf16 v[44:47], v[16:19], v[68:71], v[44:47]\n"
  "ds_read_b128 v[16:19], v0 offset:10240\n"
  "s_waitcnt lgkmcnt(3)\n"
  "v_mfma_f32_16x16x32_bf16 v[40:43], v[20:23], v[72:75], v[40:43]\n"
  "v_mfma_f32_16x16x32_bf16 v[40:43], v[24:27], v[76:79], v[40:43]\n"
  "v_mfma_f32_16x16x32_bf16 v[48:51], v[20:23], v[96:99], v[48:51]\n"
  "ds_read_b128 v[20:23], v0 offset:10752\n"
  "v_mfma_f32_16x16x32_bf16 v[48:51], v[24:27], v[100:103], v[48:51]\n"
  "v_mfma_f32_16x16x32_bf16 v[52:55], v[28:31], v[96:99], v[52:55]\n"
  "v_mfma_f32_16x16x32_bf16 v[52:55], v[32:35], v[100:103], v[52:55]\n"
  "v_mfma_f32_16x16x32_bf16 v[44:47], v[28:31], v[72:75], v[44:47]\n"
  "v_mfma_f32_16x16x32_bf16 v[44:47], v[32:35], v[76:79], v[44:47]\n"
  "s_waitcnt lgkmcnt(0)\n"
  "v_mfma_f32_16x16x32_bf16 v[40:43], v[36:39], v[80:83], v[40:43]\n"
  "v_mfma_f32_16x16x32_bf16 v[40:43], v[12:15], v[84:87], v[40:43]\n"
  "v_mfma_f32_16x16x32_bf16 v[48:51], v[36:39], v[104:107], v[48:51]\n"
  "v_mfma_f32_16x16x32_bf16 v[48:51], v[12:15], v[108:111], v[48:51]\n"
  "v_mfma_f32_16x16x32_bf16 v[52:55], v[16:19], v[104:107], v[52:55]\n"
  "v_mfma_f32_16x16x32_bf16 v[52:55], v[20:23], v[108:111], v[52:55]\n"
  "v_mfma_f32_16x16x32_bf16 v[44:47], v[16:19], v[80:83], v[44:47]\n"
  "v_mfma_f32_16x16x32_bf16 v[44:47], v[20:23], v[84:87], v[44:47]\n"
  "s_add_u32 s50, s50, 1\n"
  "s_bitcmp0_b32 s79, s50\n"
  "s_cbranch_scc1 .LH_loopb\n"
  "s_and_b32 s62, s50, 15\n"
  "s_cmp_eq_u32 s62, 15\n"
  "s_cbranch_scc1 .LH_t15b\n"
  "s_add_u32 m0, s66, 159744\n"
  "s_nop 0\n"
  "buffer_load_dword v5, s[40:43], s73 offen lds\n"
  "s_add_u32 s73, s73, 65536\n"
  "s_branch .LH_loopb\n"
  ".LH_t15b:\n"
  "s_barrier\n"
  "s_and_b32 s62, s50, 3\n"
  "s_cmp_eq_u32 s62, 3\n"
  "s_cselect_b32 s54, s61, s60\n"
  "s_cmp_eq_u32 s62, 0\n"
  "s_cselect_b32 s55, s61, s60\n"
  "v_add_u32_e32 v0, s54, v0\n"
  "v_add_u32_e32 v1, s55, v1\n"
  "v_add_u32_e32 v2, s55, v2\n"
  "s_add_u32 s72, s72, 141312\n"
  "s_add_u32 s77, s77, 141312\n"
  "s_add_u32 s78, s78, 141312\n"
  "v_fma_f32 v40, v40, s52, v6\n"
  "v_fma_f32 v41, v41, s52, v6\n"
  "buffer_load_dwordx4 v[64:67], v4, s[40:43], s72 offen offset:0 nt\n"
  "v_fma_f32 v42, v42, s52, v6\n"
  "v_fma_f32 v43, v43, s52, v6\n"
  "v_fma_f32 v48, v48, s52, v8\n"
  "v_fma_f32 v49, v49, s52, v8\n"
  "v_fma_f32 v50, v50, s52, v8\n"
  "v_fma_f32 v51, v51, s52, v8\n"
  "buffer_load_dwordx4 v[68:71], v4, s[40:43], s72 offen offset:3072 nt\n"
  "v_fma_f32 v52, v52, s52, v8\n"
  "v_fma_f32 v53, v53, s52, v8\n"
  "v_fma_f32 v54, v54, s52, v8\n"
  "v_fma_f32 v55, v55, s52, v8\n"
  "v_fma_f32 v44, v44, s52, v6\n"
  "v_fma_f32 v45, v45, s52, v6\n"
  "buffer_load_dwordx4 v[88:91], v4, s[40:43], s77 offen offset:0 nt\n"
  "v_fma_f32 v46, v46, s52, v6\n"
  "v_fma_f32 v47, v47, s52, v6\n"
  "v_exp_f32_e32 v12, v40\n"
  "v_exp_f32_e32 v13, v41\n"
  "v_exp_f32_e32 v14, v42\n"
  "buffer_load_dwordx4 v[92:95], v4, s[40:43], s77 offen offset:3072 nt\n"
  "v_exp_f32_e32 v15, v43\n"
  "v_exp_f32_e32 v16, v44\n"
  "v_exp_f32_e32 v17, v45\n"
  "v_exp_f32_e32 v18, v46\n"
  "v_exp_f32_e32 v19, v47\n"
  "v_exp_f32_e32 v20, v48\n"
  "buffer_load_dwordx4 v[72:75], v4, s[40:43], s72 offen offset:128 nt\n"
  "v_exp_f32_e32 v21, v49\n"
  "v_exp_f32_e32 v22, v50\n"
  "v_exp_f32_e32 v23, v51\n"
  "v_exp_f32_e32 v24, v52\n"
  "v_exp_f32_e32 v25, v53\n"
  "v_exp_f32_e32 v26, v54\n"
  "buffer_load_dwordx4 v[76:79], v4, s[40:43], s72 offen offset:3200 nt\n"
  "v_exp_f32_e32 v27, v55\n"
  "v_add_f32_e32 v36, v12, v13\n"
  "v_add_f32_e32 v37, v20, v21\n"
  "v_add_f32_e32 v36, v36, v14\n"
  "v_add_f32_e32 v37, v37, v22\n"
  "buffer_load_dwordx4 v[96:99], v4, s[40:43], s77 offen offset:128 nt\n"
  "v_add_f32_e32 v36, v36, v15\n"
  "v_add_f32_e32 v37, v37, v23\n"
  "v_add_f32_e32 v36, v36, v16\n"
  "v_add_f32_e32 v37, v37, v24\n"
  "v_add_f32_e32 v36, v36, v17\n"
  "v_add_f32_e32 v37, v37, v25\n"
  "buffer_load_dwordx4 v[100:103], v4, s[40:43], s77 offen offset:3200 nt\n"
  "v_add_f32_e32 v36, v36, v18\n"
  "v_add_f32_e32 v37, v37, v26\n"
  "v_add_f32_e32 v36, v36, v19\n"
  "v_add_f32_e32 v37, v37, v27\n"
  "v_max_f32_e32 v39, v36, v37\n"
  "v_cmp_lt_f32_e32 vcc, s53, v39\n"
  "s_cbranch_vccnz .LH_slowb\n"
  ".LH_slow_ret15b:\n"
  "buffer_load_dwordx4 v[80:83], v4, s[40:43], s72 offen offset:256 nt\n"
  "v_add_f32_e32 v7, v7, v36\n"
  "v_add_f32_e32 v9, v9, v37\n"
  "v_cvt_pk_bf16_f32 v40, v12, v13\n"
  "buffer_load_dwordx4 v[84:87], v4, s[40:43], s72 offen offset:3328 nt\n"
  "v_cvt_pk_bf16_f32 v41, v14, v15\n"
  "v_cvt_pk_bf16_f32 v42, v16, v17\n"
  "v_cvt_pk_bf16_f32 v43, v18, v19\n"
  "v_cvt_pk_bf16_f32 v48, v20, v21\n"
  "v_cvt_pk_bf16_f32 v49, v22, v23\n"
  "v_cvt_pk_bf16_f32 v50, v24, v25\n"
  "buffer_load_dwordx4 v[104:107], v4, s[40:43], s77 offen offset:256 nt\n"
  "v_cvt_pk_bf16_f32 v51, v26, v27\n"
  "ds_read_b64_tr_b16 v[12:13], v1 offset:0\n"
  "ds_read_b64_tr_b16 v[14:15], v1 offset:2048\n"
  "ds_read_b64_tr_b16 v[16:17], v2 offset:0\n"
  "ds_read_b64_tr_b16 v[18:19], v2 offset:2048\n"
  "ds_read_b64_tr_b16 v[20:21], v1 offset:512\n"
  "buffer_load_dwordx4 v[108:111], v4, s[40:43], s77 offen offset:3328 nt\n"
  "ds_read_b64_tr_b16 v[22:23], v1 offset:2560\n"
  "ds_read_b64_tr_b16 v[24:25], v2 offset:512\n"
  "ds_read_b64_tr_b16 v[26:27], v2 offset:2560\n"
  "v_mov_b32_e32 v44, v7\n"
  "v_mov_b32_e32 v45, v9\n"
  "s_nop 1\n"
  "v_permlane32_swap_b32_e32 v7, v44\n"
  "v_permlane32_swap_b32_e32 v9, v45\n"
  "s_add_u32 m0, s49, 0\n"
  "s_nop 0\n"
  "buffer_load_dwordx4 v4, s[36:39], s48 offen offset:0 nt lds\n"
  "s_nop 1\n"
  "v_add_f32_e32 v7, v7, v44\n"
  "v_add_f32_e32 v9, v9, v45\n"
  "v_mov_b32_e32 v44, v7\n"
  "v_mov_b32_e32 v45, v9\n"
  "s_nop 1\n"
  "v_permlane16_swap_b32_e32 v7, v44\n"
  "v_permlane16_swap_b32_e32 v9, v45\n"
  "s_nop 1\n"
  "v_add_f32_e32 v7, v7, v44\n"
  "v_add_f32_e32 v9, v9, v45\n"
  "s_add_u32 m0, s49, 3968\n"
  "s_nop 0\n"
  "buffer_load_dwordx4 v4, s[36:39], s48 offen offset:128 nt lds\n"
  "v_rcp_f32_e32 v32, v7\n"
  "v_rcp_f32_e32 v34, v9\n"
  "v_mbcnt_lo_u32_b32 v52, -1, 0\n"
  "v_mbcnt_hi_u32_b32 v52, -1, v52\n"
  "v_and_b32_e32 v53, 15, v52\n"
  "v_mul_u32_u24_e32 v33, 136, v53\n"
  "v_lshrrev_b32_e32 v53, 4, v52\n"
  "v_lshl_add_u32 v33, v53, 3, v33\n"
  "v_add_u32_e32 v33, s58, v33\n"
  "v_lshrrev_b32_e32 v53, 3, v52\n"
  "s_add_u32 m0, s49, 7936\n"
  "s_nop 0\n"
  "buffer_load_dwordx4 v4, s[36:39], s48 offen offset:256 nt lds\n"
  "v_and_b32_e32 v52, 7, v52\n"
  "v_lshlrev_b32_e32 v52, 4, v52\n"
  "v_mad_u32_u24 v35, v53, s65, v52\n"
  "v_add_u32_e32 v35, s58, v35\n"
  "v_mul_u32_u24_e32 v53, 384, v53\n"
  "v_add_u32_e32 v37, v53, v52\n"
  "s_add_u32 s49, s49, s55\n"
  "s_add_u32 s48, s48, s60\n"
  "s_cmp_eq_u32 s50, 31\n"
  "s_cbranch_scc1 .LH_finb\n"
  "s_waitcnt vmcnt(18)\n"
  "s_barrier\n"
  "s_waitcnt lgkmcnt(2)\n"
  "v_mfma_f32_16x16x32_bf16 v[112:115], v[12:15], v[40:43], v[112:115]\n"
  "ds_read_b64_tr_b16 v[28:29], v1 offset:4096\n"
  "v_mfma_f32_16x16x32_bf16 v[160:163], v[12:15], v[48:51], v[160:163]\n"
  "ds_read_b64_tr_b16 v[30:31], v1 offset:6144\n"
  "v_mfma_f32_16x16x32_bf16 v[164:167], v[16:19], v[48:51], v[164:167]\n"
  "ds_read_b64_tr_b16 v[12:13], v2 offset:4096\n"
  "v_mfma_f32_16x16x32_bf16 v[116:119], v[16:19], v[40:43], v[116:119]\n"
  "ds_read_b64_tr_b16 v[14:15], v2 offset:6144\n"
  "v_mfma_f32_16x16x32_bf16 v[120:123], v[20:23], v[40:43], v[120:123]\n"
  "ds_read_b64_tr_b16 v[16:17], v1 offset:4608\n"
  "v_mfma_f32_16x16x32_bf16 v[168:171], v[20:23], v[48:51], v[168:171]\n"
  "ds_read_b64_tr_b16 v[18:19], v1 offset:6656\n"
  "v_mul_f32_e32 v112, v32, v112\n"
  "s_waitcnt lgkmcnt(2)\n"
  "v_mfma_f32_16x16x32_bf16 v[172:175], v[24:27], v[48:51], v[172:175]\n"
  "ds_read_b64_tr_b16 v[20:21], v2 offset:4608\n"
  "v_mul_f32_e32 v113, v32, v113\n"
  "v_mfma_f32_16x16x32_bf16 v[124:127], v[24:27], v[40:43], v[124:127]\n"
  "ds_read_b64_tr_b16 v[22:23], v2 offset:6656\n"
  "v_mul_f32_e32 v114, v32, v114\n"
  "v_mfma_f32_16x16x32_bf16 v[128:131], v[28:31], v[40:43], v[128:131]\n"
  "ds_read_b64_tr_b16 v[24:25], v1 offset:8192\n"
  "v_mul_f32_e32 v115, v32, v115\n"
  "v_mfma_f32_16x16x32_bf16 v[176:179], v[28:31], v[48:51], v[176:179]\n"
  "ds_read_b64_tr_b16 v[26:27], v1 offset:10240\n"
  "v_cvt_pk_bf16_f32 v112, v112, v113\n"
  "v_mfma_f32_16x16x32_bf16 v[180:183], v[12:15], v[48:51], v[180:183]\n"
  "ds_read_b64_tr_b16 v[28:29], v2 offset:8192\n"
  "v_cvt_pk_bf16_f32 v113, v114, v115\n"
  "v_mfma_f32_16x16x32_bf16 v[132:135], v[12:15], v[40:43], v[132:135]\n"
  "ds_read_b64_tr_b16 v[30:31], v2 offset:10240\n"
  "ds_write_b64 v33, v[112:113] offset:0\n"
  "s_waitcnt lgkmcnt(3)\n"
  "v_mfma_f32_16x16x32_bf16 v[136:139], v[16:19], v[40:43], v[136:139]\n"
  "ds_read_b64_tr_b16 v[12:13], v1 offset:8704\n"
  "v_mul_f32_e32 v116, v32, v116\n"
  "v_mfma_f32_16x16x32_bf16 v[184:187], v[16:19], v[48:51], v[184:187]\n"
  "ds_read_b64_tr_b16 v[14:15], v1 offset:10752\n"
  "v_mul_f32_e32 v117, v32, v117\n"
  "v_mfma_f32_16x16x32_bf16 v[188:191], v[20:23], v[48:51], v[188:191]\n"
  "ds_read_b64_tr_b16 v[16:17], v2 offset:8704\n"
  "v_mul_f32_e32 v118, v32, v118\n"
  "v_mfma_f32_16x16x32_bf16 v[140:143], v[20:23], v[40:43], v[140:143]\n"
  "ds_read_b64_tr_b16 v[18:19], v2 offset:10752\n"
  "v_mul_f32_e32 v119, v32, v119\n"
  "v_mfma_f32_16x16x32_bf16 v[144:147], v[24:27], v[40:43], v[144:147]\n"
  "ds_read_b128 v[20:23], v0 offset:0\n"
  "v_cvt_pk_bf16_f32 v116, v116, v117\n"
  "v_mfma_f32_16x16x32_bf16 v[192:195], v[24:27], v[48:51], v[192:195]\n"
  "ds_read_b128 v[24:27], v0 offset:2048\n"
  "v_cvt_pk_bf16_f32 v117, v118, v119\n"
  "s_waitcnt lgkmcnt(2)\n"
  "v_mfma_f32_16x16x32_bf16 v[196:199], v[28:31], v[48:51], v[196:199]\n"
  "ds_write_b64 v33, v[116:117] offset:32\n"
  "v_mul_f32_e32 v120, v32, v120\n"
  "v_mfma_f32_16x16x32_bf16 v[148:151], v[28:31], v[40:43], v[148:151]\n"
  "ds_read_b128 v[28:31], v0 offset:512\n"
  "v_mul_f32_e32 v121, v32, v121\n"
  "v_mfma_f32_16x16x32_bf16 v[152:155], v[12:15], v[40:43], v[152:155]\n"
  "v_mul_f32_e32 v122, v32, v122\n"
  "v_mul_f32_e32 v123, v32, v123\n"
  "v_cvt_pk_bf16_f32 v120, v120, v121\n"
  "v_cvt_pk_bf16_f32 v121, v122, v123\n"
  "ds_write_b64 v33, v[120:121] offset:64\n"
  "v_mul_f32_e32 v124, v32, v124\n"
  "v_mul_f32_e32 v125, v32, v125\n"
  "v_mul_f32_e32 v126, v32, v126\n"
  "v_mul_f32_e32 v127, v32, v127\n"
  "v_cvt_pk_bf16_f32 v124, v124, v125\n"
  "v_cvt_pk_bf16_f32 v125, v126, v127\n"
  "ds_write_b64 v33, v[124:125] offset:96\n"
  "ds_read2_b64 v[112:115], v35 offset0:0 offset1:1\n"
  "ds_read2_b64 v[116:119], v35 offset0:136 offset1:137\n"
  "s_waitcnt lgkmcnt(1)\n"
  "buffer_store_dwordx4 v[112:115], v37, s[44:47], s56 offen offset:0 sc1\n"
  "s_waitcnt lgkmcnt(0)\n"
  "buffer_store_dwordx4 v[116:119], v37, s[44:47], s56 offen offset:3072 sc1\n"
  "v_mbcnt_lo_u32_b32 v38, -1, 0\n"
  "v_mbcnt_hi_u32_b32 v38, -1, v38\n"
  "v_lshlrev_b32_e32 v38, 4, v38\n"
  "v_add_u32_e32 v38, s58, v38\n"
  "s_sub_u32 s62, s58, s57\n"
  "v_add_u32_e32 v39, s62, v3\n"
  "s_waitcnt vmcnt(13)\n"
  "ds_write_b128 v38, v[64:67]\n"
  "ds_write_b128 v38, v[68:71] offset:1024\n"
  "ds_read_b128 v[64:67], v39\n"
  "ds_read_b128 v[68:71], v39 offset:512\n"
  "ds_write_b128 v38, v[88:91]\n"
  "ds_write_b128 v38, v[92:95] offset:1024\n"
  "ds_read_b128 v[88:91], v39\n"
  "ds_read_b128 v[92:95], v39 offset:512\n"
  "v_mfma_f32_16x16x32_bf16 v[200:203], v[12:15], v[48:51], v[200:203]\n"
  "ds_read_b128 v[12:15], v0 offset:2560\n"
  "v_mfma_f32_16x16x32_bf16 v[204:207], v[16:19], v[48:51], v[204:207]\n"
  "s_waitcnt vmcnt(9)\n"
  "ds_write_b128 v38, v[72:75]\n"
  "v_mfma_f32_16x16x32_bf16 v[156:159], v[16:19], v[40:43], v[156:159]\n"
  "ds_read_b128 v[16:19], v0 offset:4096\n"
  "ds_write_b128 v38, v[76:79] offset:1024\n"
  "s_waitcnt lgkmcnt(9)\n"
  "v_mfma_f32_16x16x32_bf16 v[40:43], v[20:23], v[64:67], 0\n"
  "ds_read_b128 v[72:75], v39\n"
  "ds_read_b128 v[76:79], v39 offset:512\n"
  "s_waitcnt lgkmcnt(5)\n"
  "v_mfma_f32_16x16x32_bf16 v[48:51], v[20:23], v[88:91], 0\n"
  "ds_read_b128 v[20:23], v0 offset:6144\n"
  "ds_write_b128 v38, v[96:99]\n"
  "v_mfma_f32_16x16x32_bf16 v[52:55], v[24:27], v[88:91], 0\n"
  "ds_write_b128 v38, v[100:103] offset:1024\n"
  "ds_read_b128 v[96:99], v39\n"
  "v_mfma_f32_16x16x32_bf16 v[44:47], v[24:27], v[64:67], 0\n"
  "ds_read_b128 v[24:27], v0 offset:4608\n"
  "ds_read_b128 v[100:103], v39 offset:512\n"
  "v_mfma_f32_16x16x32_bf16 v[40:43], v[28:31], v[68:71], v[40:43]\n"
  "v_mfma_f32_16x16x32_bf16 v[48:51], v[28:31], v[92:95], v[48:51]\n"
  "ds_read_b128 v[28:31], v0 offset:6656\n"
  "v_mfma_f32_16x16x32_bf16 v[52:55], v[12:15], v[92:95], v[52:55]\n"
  "s_waitcnt vmcnt(5)\n"
  "ds_write_b128 v38, v[80:83]\n"
  "v_mfma_f32_16x16x32_bf16 v[44:47], v[12:15], v[68:71], v[44:47]\n"
  "ds_read_b128 v[12:15], v0 offset:8192\n"
  "ds_write_b128 v38, v[84:87] offset:1024\n"
  "s_waitcnt lgkmcnt(5)\n"
  "v_mfma_f32_16x16x32_bf16 v[40:43], v[16:19], v[72:75], v[40:43]\n"
  "ds_read_b128 v[80:83], v39\n"
  "ds_read_b128 v[84:87], v39 offset:512\n"
  "v_mfma_f32_16x16x32_bf16 v[48:51], v[16:19], v[96:99], v[48:51]\n"
  "ds_read_b128 v[16:19], v0 offset:10240\n"
  "ds_write_b128 v38, v[104:107]\n"
  "v_mfma_f32_16x16x32_bf16 v[52:55], v[20:23], v[96:99], v[52:55]\n"
  "ds_write_b128 v38, v[108:111] offset:1024\n"
  "ds_read_b128 v[104:107], v39\n"
  "v_mfma_f32_16x16x32_bf16 v[44:47], v[20:23], v[72:75], v[44:47]\n"
  "ds_read_b128 v[20:23], v0 offset:8704\n"
  "ds_read_b128 v[108:111], v39 offset:512\n"
  "v_mfma_f32_16x16x32_bf16 v[40:43], v[24:27], v[76:79], v[40:43]\n"
  "v_mul_f32_e32 v160, v34, v160\n"
  "v_mul_f32_e32 v161, v34, v161\n"
  "s_waitcnt lgkmcnt(5)\n"
  "v_mfma_f32_16x16x32_bf16 v[48:51], v[24:27], v[100:103], v[48:51]\n"
  "ds_read_b128 v[24:27], v0 offset:10752\n"
  "v_mul_f32_e32 v162, v34, v162\n"
  "v_mfma_f32_16x16x32_bf16 v[52:55], v[28:31], v[100:103], v[52:55]\n"
  "v_mul_f32_e32 v163, v34, v163\n"
  "v_cvt_pk_bf16_f32 v160, v160, v161\n"
  "v_mfma_f32_16x16x32_bf16 v[44:47], v[28:31], v[76:79], v[44:47]\n"
  "v_cvt_pk_bf16_f32 v161, v162, v163\n"
  "ds_write_b64 v33, v[160:161] offset:0\n"
  "v_mfma_f32_16x16x32_bf16 v[40:43], v[12:15], v[80:83], v[40:43]\n"
  "v_mul_f32_e32 v164, v34, v164\n"
  "v_mul_f32_e32 v165, v34, v165\n"
  "s_waitcnt lgkmcnt(1)\n"
  "v_mfma_f32_16x16x32_bf16 v[48:51], v[12:15], v[104:107], v[48:51]\n"
  "v_mul_f32_e32 v166, v34, v166\n"
  "v_mul_f32_e32 v167, v34, v167\n"
  "v_mfma_f32_16x16x32_bf16 v[52:55], v[16:19], v[104:107], v[52:55]\n"
  "v_cvt_pk_bf16_f32 v164, v164, v165\n"
  "v_cvt_pk_bf16_f32 v165, v166, v167\n"
  "v_mfma_f32_16x16x32_bf16 v[44:47], v[16:19], v[80:83], v[44:47]\n"
  "ds_write_b64 v33, v[164:165] offset:32\n"
  "v_mul_f32_e32 v168, v34, v168\n"
  "v_mfma_f32_16x16x32_bf16 v[40:43], v[20:23], v[84:87], v[40:43]\n"
  "v_mul_f32_e32 v169, v34, v169\n"
  "v_mul_f32_e32 v170, v34, v170\n"
  "v_mfma_f32_16x16x32_bf16 v[48:51], v[20:23], v[108:111], v[48:51]\n"
  "v_mul_f32_e32 v171, v34, v171\n"
  "v_cvt_pk_bf16_f32 v168, v168, v169\n"
  "v_mfma_f32_16x16x32_bf16 v[52:55], v[24:27], v[108:111], v[52:55]\n"
  "v_cvt_pk_bf16_f32 v169, v170, v171\n"
  "ds_write_b64 v33, v[168:169] offset:64\n"
  "v_mfma_f32_16x16x32_bf16 v[44:47], v[24:27], v[84:87], v[44:47]\n"
  "v_mul_f32_e32 v172, v34, v172\n"
  "v_mul_f32_e32 v173, v34, v173\n"
  "s_nop 7\n"
  "s_nop 1\n"
  "v_mul_f32_e32 v174, v34, v174\n"
  "v_mul_f32_e32 v175, v34, v175\n"
  "v_cvt_pk_bf16_f32 v172, v172, v173\n"
  "v_cvt_pk_bf16_f32 v173, v174, v175\n"
  "ds_write_b64 v33, v[172:173] offset:96\n"
  "ds_read2_b64 v[160:163], v35 offset0:0 offset1:1\n"
  "ds_read2_b64 v[164:167], v35 offset0:136 offset1:137\n"
  "s_waitcnt lgkmcnt(1)\n"
  "buffer_store_dwordx4 v[160:163], v37, s[44:47], s75 offen offset:0 sc1\n"
  "s_waitcnt lgkmcnt(0)\n"
  "buffer_store_dwordx4 v[164:167], v37, s[44:47], s75 offen offset:3072 sc1\n"
  "s_mov_b32 s50, 16\n"
  "s_barrier\n"
  "s_add_u32 m0, s49, 0\n"
  "s_nop 0\n"
  "buffer_load_dwordx4 v4, s[36:39], s48 offen offset:0 nt lds\n"
  "s_add_u32 m0, s49, 3968\n"
  "s_nop 0\n"
  "buffer_load_dwordx4 v4, s[36:39], s48 offen offset:128 nt lds\n"
  "s_add_u32 m0, s49, 7936\n"
  "s_nop 0\n"
  "buffer_load_dwordx4 v4, s[36:39], s48 offen offset:256 nt lds\n"
  "s_and_b32 s62, s50, 3\n"
  "s_cmp_eq_u32 s62, 3\n"
  "s_cselect_b32 s54, s61, s60\n"
  "s_cmp_eq_u32 s62, 0\n"
  "s_cselect_b32 s55, s61, s60\n"
  "s_add_u32 s49, s49, s55\n"
  "s_add_u32 s48, s48, s60\n"
  "v_add_u32_e32 v0, s54, v0\n"
  "v_add_u32_e32 v1, s55, v1\n"
  "v_add_u32_e32 v2, s55, v2\n"
  "v_mul_f32_e32 v128, v32, v128\n"
  "v_mul_f32_e32 v129, v32, v129\n"
  "v_mul_f32_e32 v130, v32, v130\n"
  "v_mul_f32_e32 v131, v32, v131\n"
  "v_cvt_pk_bf16_f32 v128, v128, v129\n"
  "v_cvt_pk_bf16_f32 v129, v130, v131\n"
  "ds_write_b64 v33, v[128:129] offset:0\n"
  "v_mul_f32_e32 v132, v32, v132\n"
  "v_mul_f32_e32 v133, v32, v133\n"
  "v_mul_f32_e32 v134, v32, v134\n"
  "v_mul_f32_e32 v135, v32, v135\n"
  "v_cvt_pk_bf16_f32 v132, v132, v133\n"
  "v_cvt_pk_bf16_f32 v133, v134, v135\n"
  "ds_write_b64 v33, v[132:133] offset:32\n"
  "v_mul_f32_e32 v136, v32, v136\n"
  "v_mul_f32_e32 v137, v32, v137\n"
  "v_mul_f32_e32 v138, v32, v138\n"
  "v_mul_f32_e32 v139, v32, v139\n"
  "v_cvt_pk_bf16_f32 v136, v136, v137\n"
  "v_cvt_pk_bf16_f32 v137, v138, v139\n"
  "ds_write_b64 v33, v[136:137] offset:64\n"
  "v_mul_f32_e32 v140, v32, v140\n"
  "v_mul_f32_e32 v141, v32, v141\n"
  "v_mul_f32_e32 v142, v32, v142\n"
  "v_mul_f32_e32 v143, v32, v143\n"
  "v_cvt_pk_bf16_f32 v140, v140, v141\n"
  "v_cvt_pk_bf16_f32 v141, v142, v143\n"
  "ds_write_b64 v33, v[140:141] offset:96\n"
  "ds_read2_b64 v[128:131], v35 offset0:0 offset1:1\n"
  "ds_read2_b64 v[132:135], v35 offset0:136 offset1:137\n"
  "v_mul_f32_e32 v176, v34, v176\n"
  "v_mul_f32_e32 v177, v34, v177\n"
  "v_mul_f32_e32 v178, v34, v178\n"
  "v_mul_f32_e32 v179, v34, v179\n"
  "v_cvt_pk_bf16_f32 v176, v176, v177\n"
  "v_cvt_pk_bf16_f32 v177, v178, v179\n"
  "ds_write_b64 v33, v[176:177] offset:0\n"
  "v_mul_f32_e32 v180, v34, v180\n"
  "v_mul_f32_e32 v181, v34, v181\n"
  "s_waitcnt lgkmcnt(2)\n"
  "buffer_store_dwordx4 v[128:131], v37, s[44:47], s56 offen offset:128 sc1\n"
  "v_mul_f32_e32 v182, v34, v182\n"
  "v_mul_f32_e32 v183, v34, v183\n"
  "v_cvt_pk_bf16_f32 v180, v180, v181\n"
  "v_cvt_pk_bf16_f32 v181, v182, v183\n"
  "s_waitcnt lgkmcnt(1)\n"
  "buffer_store_dwordx4 v[132:135], v37, s[44:47], s56 offen offset:3200 sc1\n"
  "ds_write_b64 v33, v[180:181] offset:32\n"
  "v_mul_f32_e32 v184, v34, v184\n"
  "v_mul_f32_e32 v185, v34, v185\n"
  "v_mul_f32_e32 v186, v34, v186\n"
  "v_mul_f32_e32 v187, v34, v187\n"
  "v_cvt_pk_bf16_f32 v184, v184, v185\n"
  "v_cvt_pk_bf16_f32 v185, v186, v187\n"
  "ds_write_b64 v33, v[184:185] offset:64\n"
  "v_mul_f32_e32 v188, v34, v188\n"
  "v_mul_f32_e32 v189, v34, v189\n"
  "v_mul_f32_e32 v190, v34, v190\n"
  "v_mul_f32_e32 v191, v34, v191\n"
  "v_cvt_pk_bf16_f32 v188, v188, v189\n"
  "v_cvt_pk_bf16_f32 v189, v190, v191\n"
  "ds_write_b64 v33, v[188:189] offset:96\n"
  "ds_read2_b64 v[176:179], v35 offset0:0 offset1:1\n"
  "ds_read2_b64 v[180:183], v35 offset0:136 offset1:137\n"
  "v_max3_f32 v12, v40, v41, v42\n"
  "v_max3_f32 v13, v48, v49, v50\n"
  "v_max3_f32 v12, v12, v43, v44\n"
  "v_max3_f32 v13, v13, v51, v52\n"
  "v_max3_f32 v12, v12, v45, v46\n"
  "v_max3_f32 v13, v13, v53, v54\n"
  "v_max_f32_e32 v12, v12, v47\n"
  "v_max_f32_e32 v13, v13, v55\n"
  "v_mov_b32_e32 v15, v12\n"
  "v_mov_b32_e32 v16, v13\n"
  "s_nop 1\n"
  "v_permlane32_swap_b32_e32 v12, v15\n"
  "v_permlane32_swap_b32_e32 v13, v16\n"
  "s_nop 1\n"
  "v_max_f32_e32 v12, v12, v15\n"
  "v_max_f32_e32 v13, v13, v16\n"
  "v_mov_b32_e32 v15, v12\n"
  "v_mov_b32_e32 v16, v13\n"
  "s_nop 1\n"
  "v_permlane16_swap_b32_e32 v12, v15\n"
  "v_permlane16_swap_b32_e32 v13, v16\n"
  "s_nop 1\n"
  "v_max_f32_e32 v12, v12, v15\n"
  "v_max_f32_e32 v13, v13, v16\n"
  "v_mul_f32_e64 v6, -v12, s52\n"
  "v_mul_f32_e64 v8, -v13, s52\n"
  "v_fma_f32 v40, v40, s52, v6\n"
  "v_fma_f32 v41, v41, s52, v6\n"
  "v_fma_f32 v42, v42, s52, v6\n"
  "v_fma_f32 v43, v43, s52, v6\n"
  "v_fma_f32 v48, v48, s52, v8\n"
  "v_fma_f32 v49, v49, s52, v8\n"
  "v_fma_f32 v50, v50, s52, v8\n"
  "v_fma_f32 v51, v51, s52, v8\n"
  "v_fma_f32 v52, v52, s52, v8\n"
  "v_fma_f32 v53, v53, s52, v8\n"
  "v_fma_f32 v54, v54, s52, v8\n"
  "v_fma_f32 v55, v55, s52, v8\n"
  "v_fma_f32 v44, v44, s52, v6\n"
  "v_fma_f32 v45, v45, s52, v6\n"
  "v_fma_f32 v46, v46, s52, v6\n"
  "v_fma_f32 v47, v47, s52, v6\n"
  "v_exp_f32_e32 v40, v40\n"
  "v_exp_f32_e32 v41, v41\n"
  "v_exp_f32_e32 v42, v42\n"
  "v_exp_f32_e32 v43, v43\n"
  "v_exp_f32_e32 v44, v44\n"
  "v_exp_f32_e32 v45, v45\n"
  "v_exp_f32_e32 v46, v46\n"
  "v_exp_f32_e32 v47, v47\n"
  "v_exp_f32_e32 v48, v48\n"
  "v_exp_f32_e32 v49, v49\n"
  "v_exp_f32_e32 v50, v50\n"
  "v_exp_f32_e32 v51, v51\n"
  "v_exp_f32_e32 v52, v52\n"
  "v_exp_f32_e32 v53, v53\n"
  "v_exp_f32_e32 v54, v54\n"
  "v_exp_f32_e32 v55, v55\n"
  "v_add_f32_e32 v7, v40, v41\n"
  "v_add_f32_e32 v9, v48, v49\n"
  "v_add_f32_e32 v7, v7, v42\n"
  "v_add_f32_e32 v9, v9, v50\n"
  "v_add_f32_e32 v7, v7, v43\n"
  "v_add_f32_e32 v9, v9, v51\n"
  "v_add_f32_e32 v7, v7, v44\n"
  "v_add_f32_e32 v9, v9, v52\n"
  "v_add_f32_e32 v7, v7, v45\n"
  "v_add_f32_e32 v9, v9, v53\n"
  "v_add_f32_e32 v7, v7, v46\n"
  "v_add_f32_e32 v9, v9, v54\n"
  "v_add_f32_e32 v7, v7, v47\n"
  "v_add_f32_e32 v9, v9, v55\n"
  "v_cvt_pk_bf16_f32 v40, v40, v41\n"
  "v_cvt_pk_bf16_f32 v41, v42, v43\n"
  "v_cvt_pk_bf16_f32 v42, v44, v45\n"
  "v_cvt_pk_bf16_f32 v43, v46, v47\n"
  "v_cvt_pk_bf16_f32 v48, v48, v49\n"
  "v_cvt_pk_bf16_f32 v49, v50, v51\n"
  "v_cvt_pk_bf16_f32 v50, v52, v53\n"
  "v_cvt_pk_bf16_f32 v51, v54, v55\n"
  "s_waitcnt lgkmcnt(1)\n"
  "buffer_store_dwordx4 v[176:179], v37, s[44:47], s75 offen offset:128 sc1\n"
  "s_waitcnt lgkmcnt(0)\n"
  "buffer_store_dwordx4 v[180:183], v37, s[44:47], s75 offen offset:3200 sc1\n"
  "ds_read_b64_tr_b16 v[12:13], v1 offset:0\n"
  "ds_read_b64_tr_b16 v[14:15], v1 offset:2048\n"
  "ds_read_b64_tr_b16 v[16:17], v2 offset:0\n"
  "ds_read_b64_tr_b16 v[18:19], v2 offset:2048\n"
  "ds_read_b64_tr_b16 v[20:21], v1 offset:512\n"
  "ds_read_b64_tr_b16 v[22:23], v1 offset:2560\n"
  "ds_read_b64_tr_b16 v[24:25], v2 offset:512\n"
  "ds_read_b64_tr_b16 v[26:27], v2 offset:2560\n"
  "s_barrier\n"
  "s_waitcnt lgkmcnt(2)\n"
  "v_mfma_f32_16x16x32_bf16 v[112:115], v[12:15], v[40:43], 0\n"
  "ds_read_b64_tr_b16 v[28:29], v1 offset:4096\n"
  "v_mul_f32_e32 v144, v32, v144\n"
  "v_mul_f32_e32 v145, v32, v145\n"
  "v_mul_f32_e32 v146, v32, v146\n"
  "v_mfma_f32_16x16x32_bf16 v[160:163], v[12:15], v[48:51], 0\n"
  "ds_read_b64_tr_b16 v[30:31], v1 offset:6144\n"
  "v_mul_f32_e32 v147, v32, v147\n"
  "v_cvt_pk_bf16_f32 v144, v144, v145\n"
  "v_cvt_pk_bf16_f32 v145, v146, v147\n"
  "v_mfma_f32_16x16x32_bf16 v[164:167], v[16:19], v[48:51], 0\n"
  "ds_read_b64_tr_b16 v[12:13], v2 offset:4096\n"
  "ds_write_b64 v33, v[144:145] offset:0\n"
  "v_mul_f32_e32 v148, v32, v148\n"
  "v_mul_f32_e32 v149, v32, v149\n"
  "v_mfma_f32_16x16x32_bf16 v[116:119], v[16:19], v[40:43], 0\n"
  "ds_read_b64_tr_b16 v[14:15], v2 offset:6144\n"
  "v_mul_f32_e32 v150, v32, v150\n"
  "v_mul_f32_e32 v151, v32, v151\n"
  "v_cvt_pk_bf16_f32 v148, v148, v149\n"
  "v_mfma_f32_16x16x32_bf16 v[120:123], v[20:23], v[40:43], 0\n"
  "ds_read_b64_tr_b16 v[16:17], v1 offset:4608\n"
  "v_cvt_pk_bf16_f32 v149, v150, v151\n"
  "ds_write_b64 v33, v[148:149] offset:32\n"
  "v_mul_f32_e32 v152, v32, v152\n"
  "v_mfma_f32_16x16x32_bf16 v[168:171], v[20:23], v[48:51], 0\n"
  "ds_read_b64_tr_b16 v[18:19], v1 offset:6656\n"
  "v_mul_f32_e32 v153, v32, v153\n"
  "v_mul_f32_e32 v154, v32, v154\n"
  "v_mul_f32_e32 v155, v32, v155\n"
  "s_waitcnt lgkmcnt(3)\n"
  "v_mfma_f32_16x16x32_bf16 v[172:175], v[24:27], v[48:51], 0\n"
  "ds_read_b64_tr_b16 v[20:21], v2 offset:4608\n"
  "v_cvt_pk_bf16_f32 v152, v152, v153\n"
  "v_cvt_pk_bf16_f32 v153, v154, v155\n"
  "ds_write_b64 v33, v[152:153] offset:64\n"
  "v_mfma_f32_16x16x32_bf16 v[124:127], v[24:27], v[40:43], 0\n"
  "ds_read_b64_tr_b16 v[22:23], v2 offset:6656\n"
  "v_mul_f32_e32 v156, v32, v156\n"
  "v_mul_f32_e32 v157, v32, v157\n"
  "v_mul_f32_e32 v158, v32, v158\n"
  "v_mfma_f32_16x16x32_bf16 v[128:131], v[28:31], v[40:43], 0\n"
  "ds_read_b64_tr_b16 v[24:25], v1 offset:8192\n"
  "v_mul_f32_e32 v159, v32, v159\n"
  "v_cvt_pk_bf16_f32 v156, v156, v157\n"
  "v_cvt_pk_bf16_f32 v157, v158, v159\n"
  "v_mfma_f32_16x16x32_bf16 v[176:179], v[28:31], v[48:51], 0\n"
  "ds_read_b64_tr_b16 v[26:27], v1 offset:10240\n"
  "ds_write_b64 v33, v[156:157] offset:96\n"
  "ds_read2_b64 v[144:147], v35 offset0:0 offset1:1\n"
  "ds_read2_b64 v[148:151], v35 offset0:136 offset1:137\n"
  "v_mfma_f32_16x16x32_bf16 v[180:183], v[12:15], v[48:51], 0\n"
  "ds_read_b64_tr_b16 v[28:29], v2 offset:8192\n"
  "v_mul_f32_e32 v192, v34, v192\n"
  "v_mul_f32_e32 v193, v34, v193\n"
  "v_mul_f32_e32 v194, v34, v194\n"
  "v_mfma_f32_16x16x32_bf16 v[132:135], v[12:15], v[40:43], 0\n"
  "ds_read_b64_tr_b16 v[30:31], v2 offset:10240\n"
  "v_mul_f32_e32 v195, v34, v195\n"
  "v_cvt_pk_bf16_f32 v192, v192, v193\n"
  "v_cvt_pk_bf16_f32 v193, v194, v195\n"
  "s_waitcnt lgkmcnt(5)\n"
  "v_mfma_f32_16x16x32_bf16 v[136:139], v[16:19], v[40:43], 0\n"
  "ds_read_b64_tr_b16 v[12:13], v1 offset:8704\n"
  "ds_write_b64 v33, v[192:193] offset:0\n"
  "v_mul_f32_e32 v196, v34, v196\n"
  "v_mul_f32_e32 v197, v34, v197\n"
  "v_mfma_f32_16x16x32_bf16 v[184:187], v[16:19], v[48:51], 0\n"
  "ds_read_b64_tr_b16 v[14:15], v1 offset:10752\n"
  "s_waitcnt lgkmcnt(6)\n"
  "buffer_store_dwordx4 v[144:147], v37, s[44:47], s56 offen offset:256 sc1\n"
  "v_mul_f32_e32 v198, v34, v198\n"
  "v_mul_f32_e32 v199, v34, v199\n"
  "v_mfma_f32_16x16x32_bf16 v[188:191], v[20:23], v[48:51], 0\n"
  "ds_read_b64_tr_b16 v[16:17], v2 offset:8704\n"
  "v_cvt_pk_bf16_f32 v196, v196, v197\n"
  "v_cvt_pk_bf16_f32 v197, v198, v199\n"
  "s_waitcnt lgkmcnt(6)\n"
  "buffer_store_dwordx4 v[148:151], v37, s[44:47], s56 offen offset:3328 sc1\n"
  "v_mfma_f32_16x16x32_bf16 v[140:143], v[20:23], v[40:43], 0\n"
  "ds_read_b64_tr_b16 v[18:19], v2 offset:10752\n"
  "ds_write_b64 v33, v[196:197] offset:32\n"
  "v_mul_f32_e32 v200, v34, v200\n"
  "v_mul_f32_e32 v201, v34, v201\n"
  "v_mfma_f32_16x16x32_bf16 v[144:147], v[24:27], v[40:43], 0\n"
  "ds_read_b128 v[20:23], v0 offset:0\n"
  "v_mul_f32_e32 v202, v34, v202\n"
  "v_mul_f32_e32 v203, v34, v203\n"
  "v_cvt_pk_bf16_f32 v200, v200, v201\n"
  "v_cvt_pk_bf16_f32 v201, v202, v203\n"
  "ds_write_b64 v33, v[200:201] offset:64\n"
  "v_mul_f32_e32 v204, v34, v204\n"
  "v_mul_f32_e32 v205, v34, v205\n"
  "v_mul_f32_e32 v206, v34, v206\n"
  "v_mul_f32_e32 v207, v34, v207\n"
  "v_cvt_pk_bf16_f32 v204, v204, v205\n"
  "v_cvt_pk_bf16_f32 v205, v206, v207\n"
  "ds_write_b64 v33, v[204:205] offset:96\n"
  "ds_read2_b64 v[192:195], v35 offset0:0 offset1:1\n"
  "ds_read2_b64 v[196:199], v35 offset0:136 offset1:137\n"
  "s_waitcnt lgkmcnt(1)\n"
  "buffer_store_dwordx4 v[192:195], v37, s[44:47], s75 offen offset:256 sc1\n"
  "s_nop 1\n"
  "v_mfma_f32_16x16x32_bf16 v[192:195], v[24:27], v[48:51], 0\n"
  "ds_read_b128 v[24:27], v0 offset:2048\n"
  "s_waitcnt lgkmcnt(1)\n"
  "buffer_store_dwordx4 v[196:199], v37, s[44:47], s75 offen offset:3328 sc1\n"
  "s_nop 1\n"
  "v_mfma_f32_16x16x32_bf16 v[196:199], v[28:31], v[48:51], 0\n"
  "v_mfma_f32_16x16x32_bf16 v[148:151], v[28:31], v[40:43], 0\n"
  "ds_read_b128 v[28:31], v0 offset:512\n"
  "v_mfma_f32_16x16x32_bf16 v[152:155], v[12:15], v[40:43], 0\n"
  "v_mfma_f32_16x16x32_bf16 v[200:203], v[12:15], v[48:51], 0\n"
  "ds_read_b128 v[12:15], v0 offset:2560\n"
  "v_mfma_f32_16x16x32_bf16 v[204:207], v[16:19], v[48:51], 0\n"
  "v_mfma_f32_16x16x32_bf16 v[156:159], v[16:19], v[40:43], 0\n"
  "ds_read_b128 v[16:19], v0 offset:4096\n"
  "v_mfma_f32_16x16x32_bf16 v[40:43], v[20:23], v[64:67], 0\n"
  "v_mfma_f32_16x16x32_bf16 v[48:51], v[20:23], v[88:91], 0\n"
  "ds_read_b128 v[20:23], v0 offset:6144\n"
  "s_waitcnt lgkmcnt(2)\n"
  "v_mfma_f32_16x16x32_bf16 v[52:55], v[24:27], v[88:91], 0\n"
  "v_mfma_f32_16x16x32_bf16 v[44:47], v[24:27], v[64:67], 0\n"
  "ds_read_b128 v[24:27], v0 offset:4608\n"
  "v_mfma_f32_16x16x32_bf16 v[40:43], v[28:31], v[68:71], v[40:43]\n"
  "v_mfma_f32_16x16x32_bf16 v[48:51], v[28:31], v[92:95], v[48:51]\n"
  "ds_read_b128 v[28:31], v0 offset:6656\n"
  "v_mfma_f32_16x16x32_bf16 v[52:55], v[12:15], v[92:95], v[52:55]\n"
  "v_mfma_f32_16x16x32_bf16 v[44:47], v[12:15], v[68:71], v[44:47]\n"
  "ds_read_b128 v[12:15], v0 offset:8192\n"
  "s_waitcnt lgkmcnt(2)\n"
  "v_mfma_f32_16x16x32_bf16 v[40:43], v[16:19], v[72:75], v[40:43]\n"
  "v_mfma_f32_16x16x32_bf16 v[48:51], v[16:19], v[96:99], v[48:51]\n"
  "ds_read_b128 v[16:19], v0 offset:10240\n"
  "v_mfma_f32_16x16x32_bf16 v[52:55], v[20:23], v[96:99], v[52:55]\n"
  "v_mfma_f32_16x16x32_bf16 v[44:47], v[20:23], v[72:75], v[44:47]\n"
  "ds_read_b128 v[20:23], v0 offset:8704\n"
  "v_mfma_f32_16x16x32_bf16 v[40:43], v[24:27], v[76:79], v[40:43]\n"
  "v_mfma_f32_16x16x32_bf16 v[48:51], v[24:27], v[100:103], v[48:51]\n"
  "ds_read_b128 v[24:27], v0 offset:10752\n"
  "s_waitcnt lgkmcnt(2)\n"
  "v_mfma_f32_16x16x32_bf16 v[52:55], v[28:31], v[100:103], v[52:55]\n"
  "v_mfma_f32_16x16x32_bf16 v[44:47], v[28:31], v[76:79], v[44:47]\n"
  "v_mfma_f32_16x16x32_bf16 v[40:43], v[12:15], v[80:83], v[40:43]\n"
  "v_mfma_f32_16x16x32_bf16 v[48:51], v[12:15], v[104:107], v[48:51]\n"
  "v_mfma_f32_16x16x32_bf16 v[52:55], v[16:19], v[104:107], v[52:55]\n"
  "v_mfma_f32_16x16x32_bf16 v[44:47], v[16:19], v[80:83], v[44:47]\n"
  "s_waitcnt lgkmcnt(0)\n"
  "v_mfma_f32_16x16x32_bf16 v[40:43], v[20:23], v[84:87], v[40:43]\n"
  "v_mfma_f32_16x16x32_bf16 v[48:51], v[20:23], v[108:111], v[48:51]\n"
  "v_mfma_f32_16x16x32_bf16 v[52:55], v[24:27], v[108:111], v[52:55]\n"
  "v_mfma_f32_16x16x32_bf16 v[44:47], v[24:27], v[84:87], v[44:47]\n"
  "s_add_u32 s44, s44, 141312\n"
  "s_addc_u32 s45, s45, 0\n"
  "s_mov_b32 s50, 17\n"
  "s_branch .LH_loopb\n"
  ".LH_specialb:\n"
  "s_waitcnt vmcnt(14)\n"
  "s_branch .LH_midb\n"
  ".LH_finb:\n"
  "s_cmp_lt_u32 s59, 4\n"
  "s_cbranch_scc0 .LH_fin2b\n"
  "s_barrier\n"
  ".LH_fin2b:\n"
  "s_waitcnt lgkmcnt(2)\n"
  "v_mfma_f32_16x16x32_bf16 v[112:115], v[12:15], v[40:43], v[112:115]\n"
  "ds_read_b64_tr_b16 v[28:29], v1 offset:4096\n"
  "v_mfma_f32_16x16x32_bf16 v[160:163], v[12:15], v[48:51], v[160:163]\n"
  "ds_read_b64_tr_b16 v[30:31], v1 offset:6144\n"
  "v_mfma_f32_16x16x32_bf16 v[164:167], v[16:19], v[48:51], v[164:167]\n"
  "ds_read_b64_tr_b16 v[12:13], v2 offset:4096\n"
  "v_mfma_f32_16x16x32_bf16 v[116:119], v[16:19], v[40:43], v[116:119]\n"
  "ds_read_b64_tr_b16 v[14:15], v2 offset:6144\n"
  "v_mfma_f32_16x16x32_bf16 v[120:123], v[20:23], v[40:43], v[120:123]\n"
  "ds_read_b64_tr_b16 v[16:17], v1 offset:4608\n"
  "v_mfma_f32_16x16x32_bf16 v[168:171], v[20:23], v[48:51], v[168:171]\n"
  "ds_read_b64_tr_b16 v[18:19], v1 offset:6656\n"
  "v_mul_f32_e32 v112, v32, v112\n"
  "v_mul_f32_e32 v113, v32, v113\n"
  "s_waitcnt lgkmcnt(2)\n"
  "v_mfma_f32_16x16x32_bf16 v[172:175], v[24:27], v[48:51], v[172:175]\n"
  "ds_read_b64_tr_b16 v[20:21], v2 offset:4608\n"
  "v_mul_f32_e32 v114, v32, v114\n"
  "v_mul_f32_e32 v115, v32, v115\n"
  "v_mfma_f32_16x16x32_bf16 v[124:127], v[24:27], v[40:43], v[124:127]\n"
  "ds_read_b64_tr_b16 v[22:23], v2 offset:6656\n"
  "v_cvt_pk_bf16_f32 v112, v112, v113\n"
  "v_cvt_pk_bf16_f32 v113, v114, v115\n"
  "v_mfma_f32_16x16x32_bf16 v[128:131], v[28:31], v[40:43], v[128:131]\n"
  "ds_read_b64_tr_b16 v[24:25], v1 offset:8192\n"
  "ds_write_b64 v33, v[112:113] offset:0\n"
  "v_mul_f32_e32 v116, v32, v116\n"
  "v_mfma_f32_16x16x32_bf16 v[176:179], v[28:31], v[48:51], v[176:179]\n"
  "ds_read_b64_tr_b16 v[26:27], v1 offset:10240\n"
  "v_mul_f32_e32 v117, v32, v117\n"
  "v_mul_f32_e32 v118, v32, v118\n"
  "v_mfma_f32_16x16x32_bf16 v[180:183], v[12:15], v[48:51], v[180:183]\n"
  "ds_read_b64_tr_b16 v[28:29], v2 offset:8192\n"
  "v_mul_f32_e32 v119, v32, v119\n"
  "v_cvt_pk_bf16_f32 v116, v116, v117\n"
  "v_mfma_f32_16x16x32_bf16 v[132:135], v[12:15], v[40:43], v[132:135]\n"
  "ds_read_b64_tr_b16 v[30:31], v2 offset:10240\n"
  "v_cvt_pk_bf16_f32 v117, v118, v119\n"
  "ds_write_b64 v33, v[116:117] offset:32\n"
  "s_waitcnt lgkmcnt(3)\n"
  "v_mfma_f32_16x16x32_bf16 v[136:139], v[16:19], v[40:43], v[136:139]\n"
  "ds_read_b64_tr_b16 v[12:13], v1 offset:8704\n"
  "v_mul_f32_e32 v120, v32, v120\n"
  "v_mul_f32_e32 v121, v32, v121\n"
  "v_mfma_f32_16x16x32_bf16 v[184:187], v[16:19], v[48:51], v[184:187]\n"
  "ds_read_b64_tr_b16 v[14:15], v1 offset:10752\n"
  "v_mul_f32_e32 v122, v32, v122\n"
  "v_mul_f32_e32 v123, v32, v123\n"
  "v_mfma_f32_16x16x32_bf16 v[188:191], v[20:23], v[48:51], v[188:191]\n"
  "ds_read_b64_tr_b16 v[16:17], v2 offset:8704\n"
  "v_cvt_pk_bf16_f32 v120, v120, v121\n"
  "v_cvt_pk_bf16_f32 v121, v122, v123\n"
  "v_mfma_f32_16x16x32_bf16 v[140:143], v[20:23], v[40:43], v[140:143]\n"
  "ds_read_b64_tr_b16 v[18:19], v2 offset:10752\n"
  "ds_write_b64 v33, v[120:121] offset:64\n"
  "v_mul_f32_e32 v124, v32, v124\n"
  "v_mfma_f32_16x16x32_bf16 v[144:147], v[24:27], v[40:43], v[144:147]\n"
  "v_mul_f32_e32 v125, v32, v125\n"
  "v_mul_f32_e32 v126, v32, v126\n"
  "v_mul_f32_e32 v127, v32, v127\n"
  "v_mfma_f32_16x16x32_bf16 v[192:195], v[24:27], v[48:51], v[192:195]\n"
  "v_cvt_pk_bf16_f32 v124, v124, v125\n"
  "v_cvt_pk_bf16_f32 v125, v126, v127\n"
  "ds_write_b64 v33, v[124:125] offset:96\n"
  "s_waitcnt lgkmcnt(2)\n"
  "v_mfma_f32_16x16x32_bf16 v[196:199], v[28:31], v[48:51], v[196:199]\n"
  "ds_read2_b64 v[112:115], v35 offset0:0 offset1:1\n"
  "ds_read2_b64 v[116:119], v35 offset0:136 offset1:137\n"
  "v_mul_f32_e32 v160, v34, v160\n"
  "v_mfma_f32_16x16x32_bf16 v[148:151], v[28:31], v[40:43], v[148:151]\n"
  "v_mul_f32_e32 v161, v34, v161\n"
  "v_mul_f32_e32 v162, v34, v162\n"
  "v_mul_f32_e32 v163, v34, v163\n"
  "v_mfma_f32_16x16x32_bf16 v[152:155], v[12:15], v[40:43], v[152:155]\n"
  "v_cvt_pk_bf16_f32 v160, v160, v161\n"
  "v_cvt_pk_bf16_f32 v161, v162, v163\n"
  "ds_write_b64 v33, v[160:161] offset:0\n"
  "v_mfma_f32_16x16x32_bf16 v[200:203], v[12:15], v[48:51], v[200:203]\n"
  "v_mul_f32_e32 v164, v34, v164\n"
  "v_mul_f32_e32 v165, v34, v165\n"
  "v_mfma_f32_16x16x32_bf16 v[204:207], v[16:19], v[48:51], v[204:207]\n"
  "s_waitcnt lgkmcnt(2)\n"
  "buffer_store_dwordx4 v[112:115], v37, s[44:47], s56 offen offset:0 sc1\n"
  "v_mul_f32_e32 v166, v34, v166\n"
  "v_mul_f32_e32 v167, v34, v167\n"
  "v_mfma_f32_16x16x32_bf16 v[156:159], v[16:19], v[40:43], v[156:159]\n"
  "v_cvt_pk_bf16_f32 v164, v164, v165\n"
  "v_cvt_pk_bf16_f32 v165, v166, v167\n"
  "s_waitcnt lgkmcnt(1)\n"
  "buffer_store_dwordx4 v[116:119], v37, s[44:47], s56 offen offset:3072 sc1\n"
  "s_nop 7\n"
  "s_nop 1\n"
  "ds_write_b64 v33, v[164:165] offset:32\n"
  "v_mul_f32_e32 v168, v34, v168\n"
  "v_mul_f32_e32 v169, v34, v169\n"
  "v_mul_f32_e32 v170, v34, v170\n"
  "v_mul_f32_e32 v171, v34, v171\n"
  "v_cvt_pk_bf16_f32 v168, v168, v169\n"
  "v_cvt_pk_bf16_f32 v169, v170, v171\n"
  "ds_write_b64 v33, v[168:169] offset:64\n"
  "v_mul_f32_e32 v172, v34, v172\n"
  "v_mul_f32_e32 v173, v34, v173\n"
  "v_mul_f32_e32 v174, v34, v174\n"
  "v_mul_f32_e32 v175, v34, v175\n"
  "v_cvt_pk_bf16_f32 v172, v172, v173\n"
  "v_cvt_pk_bf16_f32 v173, v174, v175\n"
  "ds_write_b64 v33, v[172:173] offset:96\n"
  "ds_read2_b64 v[160:163], v35 offset0:0 offset1:1\n"
  "ds_read2_b64 v[164:167], v35 offset0:136 offset1:137\n"
  "v_mul_f32_e32 v128, v32, v128\n"
  "v_mul_f32_e32 v129, v32, v129\n"
  "v_mul_f32_e32 v130, v32, v130\n"
  "v_mul_f32_e32 v131, v32, v131\n"
  "v_cvt_pk_bf16_f32 v128, v128, v129\n"
  "v_cvt_pk_bf16_f32 v129, v130, v131\n"
  "ds_write_b64 v33, v[128:129] offset:0\n"
  "v_mul_f32_e32 v132, v32, v132\n"
  "v_mul_f32_e32 v133, v32, v133\n"
  "s_waitcnt lgkmcnt(2)\n"
  "buffer_store_dwordx4 v[160:163], v37, s[44:47], s75 offen offset:0 sc1\n"
  "v_mul_f32_e32 v134, v32, v134\n"
  "v_mul_f32_e32 v135, v32, v135\n"
  "v_cvt_pk_bf16_f32 v132, v132, v133\n"
  "v_cvt_pk_bf16_f32 v133, v134, v135\n"
  "s_waitcnt lgkmcnt(1)\n"
  "buffer_store_dwordx4 v[164:167], v37, s[44:47], s75 offen offset:3072 sc1\n"
  "ds_write_b64 v33, v[132:133] offset:32\n"
  "v_mul_f32_e32 v136, v32, v136\n"
  "v_mul_f32_e32 v137, v32, v137\n"
  "v_mul_f32_e32 v138, v32, v138\n"
  "v_mul_f32_e32 v139, v32, v139\n"
  "v_cvt_pk_bf16_f32 v136, v136, v137\n"
  "v_cvt_pk_bf16_f32 v137, v138, v139\n"
  "ds_write_b64 v33, v[136:137] offset:64\n"
  "v_mul_f32_e32 v140, v32, v140\n"
  "v_mul_f32_e32 v141, v32, v141\n"
  "v_mul_f32_e32 v142, v32, v142\n"
  "v_mul_f32_e32 v143, v32, v143\n"
  "v_cvt_pk_bf16_f32 v140, v140, v141\n"
  "v_cvt_pk_bf16_f32 v141, v142, v143\n"
  "ds_write_b64 v33, v[140:141] offset:96\n"
  "ds_read2_b64 v[128:131], v35 offset0:0 offset1:1\n"
  "ds_read2_b64 v[132:135], v35 offset0:136 offset1:137\n"
  "v_mul_f32_e32 v176, v34, v176\n"
  "v_mul_f32_e32 v177, v34, v177\n"
  "v_mul_f32_e32 v178, v34, v178\n"
  "v_mul_f32_e32 v179, v34, v179\n"
  "v_cvt_pk_bf16_f32 v176, v176, v177\n"
  "v_cvt_pk_bf16_f32 v177, v178, v179\n"
  "ds_write_b64 v33, v[176:177] offset:0\n"
  "v_mul_f32_e32 v180, v34, v180\n"
  "v_mul_f32_e32 v181, v34, v181\n"
  "s_waitcnt lgkmcnt(2)\n"
  "buffer_store_dwordx4 v[128:131], v37, s[44:47], s56 offen offset:128 sc1\n"
  "v_mul_f32_e32 v182, v34, v182\n"
  "v_mul_f32_e32 v183, v34, v183\n"
  "v_cvt_pk_bf16_f32 v180, v180, v181\n"
  "v_cvt_pk_bf16_f32 v181, v182, v183\n"
  "s_waitcnt lgkmcnt(1)\n"
  "buffer_store_dwordx4 v[132:135], v37, s[44:47], s56 offen offset:3200 sc1\n"
  "ds_write_b64 v33, v[180:181] offset:32\n"
  "v_mul_f32_e32 v184, v34, v184\n"
  "v_mul_f32_e32 v185, v34, v185\n"
  "v_mul_f32_e32 v186, v34, v186\n"
  "v_mul_f32_e32 v187, v34, v187\n"
  "v_cvt_pk_bf16_f32 v184, v184, v185\n"
  "v_cvt_pk_bf16_f32 v185, v186, v187\n"
  "ds_write_b64 v33, v[184:185] offset:64\n"
  "v_mul_f32_e32 v188, v34, v188\n"
  "v_mul_f32_e32 v189, v34, v189\n"
  "v_mul_f32_e32 v190, v34, v190\n"
  "v_mul_f32_e32 v191, v34, v191\n"
  "v_cvt_pk_bf16_f32 v188, v188, v189\n"
  "v_cvt_pk_bf16_f32 v189, v190, v191\n"
  "ds_write_b64 v33, v[188:189] offset:96\n"
  "ds_read2_b64 v[176:179], v35 offset0:0 offset1:1\n"
  "ds_read2_b64 v[180:183], v35 offset0:136 offset1:137\n"
  "v_mul_f32_e32 v144, v32, v144\n"
  "v_mul_f32_e32 v145, v32, v145\n"
  "v_mul_f32_e32 v146, v32, v146\n"
  "v_mul_f32_e32 v147, v32, v147\n"
  "v_cvt_pk_bf16_f32 v144, v144, v145\n"
  "v_cvt_pk_bf16_f32 v145, v146, v147\n"
  "ds_write_b64 v33, v[144:145] offset:0\n"
  "v_mul_f32_e32 v148, v32, v148\n"
  "v_mul_f32_e32 v149, v32, v149\n"
  "s_waitcnt lgkmcnt(2)\n"
  "buffer_store_dwordx4 v[176:179], v37, s[44:47], s75 offen offset:128 sc1\n"
  "v_mul_f32_e32 v150, v32, v150\n"
  "v_mul_f32_e32 v151, v32, v151\n"
  "v_cvt_pk_bf16_f32 v148, v148, v149\n"
  "v_cvt_pk_bf16_f32 v149, v150, v151\n"
  "s_waitcnt lgkmcnt(1)\n"
  "buffer_store_dwordx4 v[180:183], v37, s[44:47], s75 offen offset:3200 sc1\n"
  "ds_write_b64 v33, v[148:149] offset:32\n"
  "v_mul_f32_e32 v152, v32, v152\n"
  "v_mul_f32_e32 v153, v32, v153\n"
  "v_mul_f32_e32 v154, v32, v154\n"
  "v_mul_f32_e32 v155, v32, v155\n"
  "v_cvt_pk_bf16_f32 v152, v152, v153\n"
  "v_cvt_pk_bf16_f32 v153, v154, v155\n"
  "ds_write_b64 v33, v[152:153] offset:64\n"
  "v_mul_f32_e32 v156, v32, v156\n"
  "v_mul_f32_e32 v157, v32, v157\n"
  "v_mul_f32_e32 v158, v32, v158\n"
  "v_mul_f32_e32 v159, v32, v159\n"
  "v_cvt_pk_bf16_f32 v156, v156, v157\n"
  "v_cvt_pk_bf16_f32 v157, v158, v159\n"
  "ds_write_b64 v33, v[156:157] offset:96\n"
  "ds_read2_b64 v[144:147], v35 offset0:0 offset1:1\n"
  "ds_read2_b64 v[148:151], v35 offset0:136 offset1:137\n"
  "v_mul_f32_e32 v192, v34, v192\n"
  "v_mul_f32_e32 v193, v34, v193\n"
  "v_mul_f32_e32 v194, v34, v194\n"
  "v_mul_f32_e32 v195, v34, v195\n"
  "v_cvt_pk_bf16_f32 v192, v192, v193\n"
  "v_cvt_pk_bf16_f32 v193, v194, v195\n"
  "ds_write_b64 v33, v[192:193] offset:0\n"
  "v_mul_f32_e32 v196, v34, v196\n"
  "v_mul_f32_e32 v197, v34, v197\n"
  "s_waitcnt lgkmcnt(2)\n"
  "buffer_store_dwordx4 v[144:147], v37, s[44:47], s56 offen offset:256 sc1\n"
  "v_mul_f32_e32 v198, v34, v198\n"
  "v_mul_f32_e32 v199, v34, v199\n"
  "v_cvt_pk_bf16_f32 v196, v196, v197\n"
  "v_cvt_pk_bf16_f32 v197, v198, v199\n"
  "s_waitcnt lgkmcnt(1)\n"
  "buffer_store_dwordx4 v[148:151], v37, s[44:47], s56 offen offset:3328 sc1\n"
  "ds_write_b64 v33, v[196:197] offset:32\n"
  "v_mul_f32_e32 v200, v34, v200\n"
  "v_mul_f32_e32 v201, v34, v201\n"
  "v_mul_f32_e32 v202, v34, v202\n"
  "v_mul_f32_e32 v203, v34, v203\n"
  "v_cvt_pk_bf16_f32 v200, v200, v201\n"
  "v_cvt_pk_bf16_f32 v201, v202, v203\n"
  "ds_write_b64 v33, v[200:201] offset:64\n"
  "v_mul_f32_e32 v204, v34, v204\n"
  "v_mul_f32_e32 v205, v34, v205\n"
  "v_mul_f32_e32 v206, v34, v206\n"
  "v_mul_f32_e32 v207, v34, v207\n"
  "v_cvt_pk_bf16_f32 v204, v204, v205\n"
  "v_cvt_pk_bf16_f32 v205, v206, v207\n"
  "ds_write_b64 v33, v[204:205] offset:96\n"
  "ds_read2_b64 v[192:195], v35 offset0:0 offset1:1\n"
  "ds_read2_b64 v[196:199], v35 offset0:136 offset1:137\n"
  "s_waitcnt lgkmcnt(1)\n"
  "buffer_store_dwordx4 v[192:195], v37, s[44:47], s75 offen offset:256 sc1\n"
  "s_waitcnt lgkmcnt(0)\n"
  "buffer_store_dwordx4 v[196:199], v37, s[44:47], s75 offen offset:3328 sc1\n"
  "s_branch .LH_end\n"
  ".LH_slowb:\n"
  "v_max3_f32 v36, v40, v41, v42\n"
  "v_max3_f32 v37, v48, v49, v50\n"
  "v_max3_f32 v36, v36, v43, v44\n"
  "v_max3_f32 v37, v37, v51, v52\n"
  "v_max3_f32 v36, v36, v45, v46\n"
  "v_max3_f32 v37, v37, v53, v54\n"
  "v_max_f32_e32 v36, v36, v47\n"
  "v_max_f32_e32 v37, v37, v55\n"
  "v_mov_b32_e32 v12, v36\n"
  "v_mov_b32_e32 v13, v37\n"
  "s_nop 1\n"
  "v_permlane32_swap_b32_e32 v36, v12\n"
  "v_permlane32_swap_b32_e32 v37, v13\n"
  "s_nop 1\n"
  "v_max_f32_e32 v36, v36, v12\n"
  "v_max_f32_e32 v37, v37, v13\n"
  "v_mov_b32_e32 v12, v36\n"
  "v_mov_b32_e32 v13, v37\n"
  "s_nop 1\n"
  "v_permlane16_swap_b32_e32 v36, v12\n"
  "v_permlane16_swap_b32_e32 v37, v13\n"
  "s_nop 1\n"
  "v_max_f32_e32 v36, v36, v12\n"
  "v_max_f32_e32 v37, v37, v13\n"
  "v_max_f32_e32 v36, 0, v36\n"
  "v_max_f32_e32 v37, 0, v37\n"
  "v_sub_f32_e32 v6, v6, v36\n"
  "v_sub_f32_e32 v8, v8, v37\n"
  "v_exp_f32_e64 v15, -v36\n"
  "v_exp_f32_e64 v16, -v37\n"
  "v_sub_f32_e32 v40, v40, v36\n"
  "v_sub_f32_e32 v41, v41, v36\n"
  "v_sub_f32_e32 v42, v42, v36\n"
  "v_sub_f32_e32 v43, v43, v36\n"
  "v_sub_f32_e32 v44, v44, v36\n"
  "v_sub_f32_e32 v45, v45, v36\n"
  "v_sub_f32_e32 v46, v46, v36\n"
  "v_sub_f32_e32 v47, v47, v36\n"
  "v_sub_f32_e32 v48, v48, v37\n"
  "v_sub_f32_e32 v49, v49, v37\n"
  "v_sub_f32_e32 v50, v50, v37\n"
  "v_sub_f32_e32 v51, v51, v37\n"
  "v_sub_f32_e32 v52, v52, v37\n"
  "v_sub_f32_e32 v53, v53, v37\n"
  "v_sub_f32_e32 v54, v54, v37\n"
  "v_sub_f32_e32 v55, v55, v37\n"
  "v_mul_f32_e32 v7, v7, v15\n"
  "v_mul_f32_e32 v112, v112, v15\n"
  "v_mul_f32_e32 v113, v113, v15\n"
  "v_mul_f32_e32 v114, v114, v15\n"
  "v_mul_f32_e32 v115, v115, v15\n"
  "v_mul_f32_e32 v116, v116, v15\n"
  "v_mul_f32_e32 v117, v117, v15\n"
  "v_mul_f32_e32 v118, v118, v15\n"
  "v_mul_f32_e32 v119, v119, v15\n"
  "v_mul_f32_e32 v120, v120, v15\n"
  "v_mul_f32_e32 v121, v121, v15\n"
  "v_mul_f32_e32 v122, v122, v15\n"
  "v_mul_f32_e32 v123, v123, v15\n"
  "v_mul_f32_e32 v124, v124, v15\n"
  "v_mul_f32_e32 v125, v125, v15\n"
  "v_mul_f32_e32 v126, v126, v15\n"
  "v_mul_f32_e32 v127, v127, v15\n"
  "v_mul_f32_e32 v128, v128, v15\n"
  "v_mul_f32_e32 v129, v129, v15\n"
  "v_mul_f32_e32 v130, v130, v15\n"
  "v_mul_f32_e32 v131, v131, v15\n"
  "v_mul_f32_e32 v132, v132, v15\n"
  "v_mul_f32_e32 v133, v133, v15\n"
  "v_mul_f32_e32 v134, v134, v15\n"
  "v_mul_f32_e32 v135, v135, v15\n"
  "v_mul_f32_e32 v136, v136, v15\n"
  "v_mul_f32_e32 v137, v137, v15\n"
  "v_mul_f32_e32 v138, v138, v15\n"
  "v_mul_f32_e32 v139, v139, v15\n"
  "v_mul_f32_e32 v140, v140, v15\n"
  "v_mul_f32_e32 v141, v141, v15\n"
  "v_mul_f32_e32 v142, v142, v15\n"
  "v_mul_f32_e32 v143, v143, v15\n"
  "v_mul_f32_e32 v144, v144, v15\n"
  "v_mul_f32_e32 v145, v145, v15\n"
  "v_mul_f32_e32 v146, v146, v15\n"
  "v_mul_f32_e32 v147, v147, v15\n"
  "v_mul_f32_e32 v148, v148, v15\n"
  "v_mul_f32_e32 v149, v149, v15\n"
  "v_mul_f32_e32 v150, v150, v15\n"
  "v_mul_f32_e32 v151, v151, v15\n"
  "v_mul_f32_e32 v152, v152, v15\n"
  "v_mul_f32_e32 v153, v153, v15\n"
  "v_mul_f32_e32 v154, v154, v15\n"
  "v_mul_f32_e32 v155, v155, v15\n"
  "v_mul_f32_e32 v156, v156, v15\n"
  "v_mul_f32_e32 v157, v157, v15\n"
  "v_mul_f32_e32 v158, v158, v15\n"
  "v_mul_f32_e32 v159, v159, v15\n"
  "v_mul_f32_e32 v9, v9, v16\n"
  "v_mul_f32_e32 v160, v160, v16\n"
  "v_mul_f32_e32 v161, v161, v16\n"
  "v_mul_f32_e32 v162, v162, v16\n"
  "v_mul_f32_e32 v163, v163, v16\n"
  "v_mul_f32_e32 v164, v164, v16\n"
  "v_mul_f32_e32 v165, v165, v16\n"
  "v_mul_f32_e32 v166, v166, v16\n"
  "v_mul_f32_e32 v167, v167, v16\n"
  "v_mul_f32_e32 v168, v168, v16\n"
  "v_mul_f32_e32 v169, v169, v16\n"
  "v_mul_f32_e32 v170, v170, v16\n"
  "v_mul_f32_e32 v171, v171, v16\n"
  "v_mul_f32_e32 v172, v172, v16\n"
  "v_mul_f32_e32 v173, v173, v16\n"
  "v_mul_f32_e32 v174, v174, v16\n"
  "v_mul_f32_e32 v175, v175, v16\n"
  "v_mul_f32_e32 v176, v176, v16\n"
  "v_mul_f32_e32 v177, v177, v16\n"
  "v_mul_f32_e32 v178, v178, v16\n"
  "v_mul_f32_e32 v179, v179, v16\n"
  "v_mul_f32_e32 v180, v180, v16\n"
  "v_mul_f32_e32 v181, v181, v16\n"
  "v_mul_f32_e32 v182, v182, v16\n"
  "v_mul_f32_e32 v183, v183, v16\n"
  "v_mul_f32_e32 v184, v184, v16\n"
  "v_mul_f32_e32 v185, v185, v16\n"
  "v_mul_f32_e32 v186, v186, v16\n"
  "v_mul_f32_e32 v187, v187, v16\n"
  "v_mul_f32_e32 v188, v188, v16\n"
  "v_mul_f32_e32 v189, v189, v16\n"
  "v_mul_f32_e32 v190, v190, v16\n"
  "v_mul_f32_e32 v191, v191, v16\n"
  "v_mul_f32_e32 v192, v192, v16\n"
  "v_mul_f32_e32 v193, v193, v16\n"
  "v_mul_f32_e32 v194, v194, v16\n"
  "v_mul_f32_e32 v195, v195, v16\n"
  "v_mul_f32_e32 v196, v196, v16\n"
  "v_mul_f32_e32 v197, v197, v16\n"
  "v_mul_f32_e32 v198, v198, v16\n"
  "v_mul_f32_e32 v199, v199, v16\n"
  "v_mul_f32_e32 v200, v200, v16\n"
  "v_mul_f32_e32 v201, v201, v16\n"
  "v_mul_f32_e32 v202, v202, v16\n"
  "v_mul_f32_e32 v203, v203, v16\n"
  "v_mul_f32_e32 v204, v204, v16\n"
  "v_mul_f32_e32 v205, v205, v16\n"
  "v_mul_f32_e32 v206, v206, v16\n"
  "v_mul_f32_e32 v207, v207, v16\n"
  "v_exp_f32_e32 v12, v40\n"
  "v_exp_f32_e32 v13, v41\n"
  "v_exp_f32_e32 v14, v42\n"
  "v_exp_f32_e32 v15, v43\n"
  "v_exp_f32_e32 v16, v44\n"
  "v_exp_f32_e32 v17, v45\n"
  "v_exp_f32_e32 v18, v46\n"
  "v_exp_f32_e32 v19, v47\n"
  "v_exp_f32_e32 v20, v48\n"
  "v_exp_f32_e32 v21, v49\n"
  "v_exp_f32_e32 v22, v50\n"
  "v_exp_f32_e32 v23, v51\n"
  "v_exp_f32_e32 v24, v52\n"
  "v_exp_f32_e32 v25, v53\n"
  "v_exp_f32_e32 v26, v54\n"
  "v_exp_f32_e32 v27, v55\n"
  "v_add_f32_e32 v36, v12, v13\n"
  "v_add_f32_e32 v37, v20, v21\n"
  "v_add_f32_e32 v36, v36, v14\n"
  "v_add_f32_e32 v37, v37, v22\n"
  "v_add_f32_e32 v36, v36, v15\n"
  "v_add_f32_e32 v37, v37, v23\n"
  "v_add_f32_e32 v36, v36, v16\n"
  "v_add_f32_e32 v37, v37, v24\n"
  "v_add_f32_e32 v36, v36, v17\n"
  "v_add_f32_e32 v37, v37, v25\n"
  "v_add_f32_e32 v36, v36, v18\n"
  "v_add_f32_e32 v37, v37, v26\n"
  "v_add_f32_e32 v36, v36, v19\n"
  "v_add_f32_e32 v37, v37, v27\n"
  "s_and_b32 s63, s50, 15\n"
  "s_cmp_eq_u32 s63, 15\n"
  "s_cbranch_scc1 .LH_slow_ret15b\n"
  "s_branch .LH_slow_retb\n"
  ".LH_end:\n"
  :: "s"(q), "s"(k), "s"(v), "s"(o), "s"(wid), "s"(lb), "s"(blockIdx.x)
  : "v0", "v1", "v2", "v3", "v4", "v5", "v6", "v7", "v8", "v9", "v10", "v11", "v12", "v13", "v14", "v15", "v16", "v17", "v18", "v19", "v20", "v21", "v22", "v23", "v24", "v25", "v26", "v27", "v28", "v29", "v30", "v31", "v32", "v33", "v34", "v35", "v36", "v37", "v38", "v39", "v40", "v41", "v42", "v43", "v44", "v45", "v46", "v47", "v48", "v49", "v50", "v51", "v52", "v53", "v54", "v55", "v56", "v57", "v58", "v59", "v60", "v61", "v62", "v63", "v64", "v65", "v66", "v67", "v68", "v69", "v70", "v71", "v72", "v73", "v74", "v75", "v76", "v77", "v78", "v79", "v80", "v81", "v82", "v83", "v84", "v85", "v86", "v87", "v88", "v89", "v90", "v91", "v92", "v93", "v94", "v95", "v96", "v97", "v98", "v99", "v100", "v101", "v102", "v103", "v104", "v105", "v106", "v107", "v108", "v109", "v110", "v111", "v112", "v113", "v114", "v115", "v116", "v117", "v118", "v119", "v120", "v121", "v122", "v123", "v124", "v125", "v126", "v127", "v128", "v129", "v130", "v131", "v132", "v133", "v134", "v135", "v136", "v137", "v138", "v139", "v140", "v141", "v142", "v143", "v144", "v145", "v146", "v147", "v148", "v149", "v150", "v151", "v152", "v153", "v154", "v155", "v156", "v157", "v158", "v159", "v160", "v161", "v162", "v163", "v164", "v165", "v166", "v167", "v168", "v169", "v170", "v171", "v172", "v173", "v174", "v175", "v176", "v177", "v178", "v179", "v180", "v181", "v182", "v183", "v184", "v185", "v186", "v187", "v188", "v189", "v190", "v191", "v192", "v193", "v194", "v195", "v196", "v197", "v198", "v199", "v200", "v201", "v202", "v203", "v204", "v205", "v206", "v207", "v208", "v209", "v210", "v211", "v212", "v213", "v214", "v215", "v216", "v217", "v218", "v219", "v220", "v221", "v222", "v223", "v224", "v225", "v226", "v227", "v228", "v229", "v230", "v231", "v232", "v233", "v234", "v235", "v236", "v237", "v238", "v239", "v240", "v241", "v242", "v243", "v244", "v245", "v246", "v247", "v248", "v249", "v250", "v251", "v252", "v253", "v254", "v255", "s36", "s37", "s38", "s39", "s40", "s41", "s42", "s43", "s44", "s45", "s46", "s47", "s48", "s49", "s50", "s51", "s52", "s53", "s54", "s55", "s56", "s57", "s58", "s59", "s60", "s61", "s62", "s63", "s64", "s65", "s66", "s67", "s68", "s69", "s70", "s71", "s72", "s73", "s74", "s75", "s76", "s77", "s78", "s79", "s80", "s81", "s82", "s83", "s84", "s85", "s86", "s87", "s88", "s89", "s90", "s91", "vcc", "scc", "memory");
}

extern "C" void kernel_launch(void* const* d_in, const int* in_sizes, int n_in, void* d_out, int out_size, void* d_ws, size_t ws_size,
                              hipStream_t stream) {
  (void)in_sizes; (void)n_in; (void)out_size; (void)d_ws; (void)ws_size;
  hipLaunchKernelGGL(attn_h, dim3(256), dim3(512), 0, stream, (const char*)d_in[0], (const char*)d_in[1], (const char*)d_in[2], (char*)d_out);
}
```
